```python
import torch

def _softmax(x, axis=-1):
    return torch.softmax(x, dim=axis)
def _gelu(x):
    return torch.nn.functional.gelu(x, approximate="tanh")
def _silu(x):
    return torch.nn.functional.silu(x)
def _sigmoid(x):
    return torch.sigmoid(x)
def _layernorm(x, axis=-1, eps=1e-5):
    m = x.mean(dim=axis, keepdim=True)
    v = x.var(dim=axis, keepdim=True, unbiased=False)
    return (x - m) / torch.sqrt(v + eps)
def _scatter_add(tgt, idx, src, axis=0):
    return tgt.clone().index_add_(axis, idx, src)
def _segment_sum(src, seg, n):
    out = torch.zeros((n,) + src.shape[1:], dtype=src.dtype)
    return out.index_add_(0, seg, src)
def _pad(x, pw):
    flat = tuple(v for pair in reversed(pw) for v in pair)
    return torch.nn.functional.pad(x, flat)

kernel_name = 'standalone__weight_only_int_squeezellm__prompt'

def setup_inputs():

    _w = (lambda a: (lambda t: t.to(torch.bfloat16) if t.is_floating_point() else t)(torch.as_tensor(a)))
    B, S, D, F = 1, 2048, 576, 4608
    bits = 8 if (2 <= 8 <= 4) else 4
    L = 2 ** bits
    qw = torch.randint(0, L, (D, F)).to(torch.int64)
    lut = (lambda *s: torch.randn(*s, dtype=torch.bfloat16) * 0.05)(L, F)
    return {
        "x": _w((lambda *s: torch.randn(*s, dtype=torch.bfloat16) * 0.05)(B, S, D)),
        "qw": _w(qw),
        "lut": _w(lut),
    }

def reference(x, qw, lut):
    w = (lambda x, i, axis: torch.take_along_dim(x, i, dim=axis))(lut, qw, axis=0)
    w = (lambda x, dt: x.to(dt))(w, x.dtype)
    return torch.einsum('bsd,df->bsf', x, w)
```

```cpp
#include <hip/hip_runtime.h>
#include <hip/hip_bf16.h>
typedef __attribute__((ext_vector_type(8))) short bf16x8;
typedef __attribute__((ext_vector_type(4))) float f32x4;

__global__ __launch_bounds__(256) void k_naive(const unsigned short* __restrict__ x, const int* __restrict__ qw,
                        const unsigned short* __restrict__ lut, __hip_bfloat16* __restrict__ out, int M, int K, int N) {
  int wid = (blockIdx.x * 256 + threadIdx.x) >> 6;
  int lane = threadIdx.x & 63;
  int tn = N / 16;
  int bm = (wid / tn) * 16, bn = (wid % tn) * 16;
  int r = lane & 15, q = lane >> 4;
  f32x4 acc = {0, 0, 0, 0};
  for (int k0 = 0; k0 < K; k0 += 32) {
    bf16x8 a = *(const bf16x8*)(x + (long)(bm + r) * K + k0 + q * 8);
    bf16x8 b;
    for (int j = 0; j < 8; ++j) {
      int c = qw[(long)(k0 + q * 8 + j) * N + bn + r];
      b[j] = (short)lut[(long)c * N + bn + r];
    }
    acc = __builtin_amdgcn_mfma_f32_16x16x32_bf16(a, b, acc, 0, 0, 0);
  }
  for (int j = 0; j < 4; ++j) out[(long)(bm + q * 4 + j) * N + bn + r] = __float2bfloat16(acc[j]);
}

extern "C" void kernel_launch(void* const* d_in, const int* in_sizes, int n_in,
                              void* d_out, int out_size, void* d_ws, size_t ws_size,
                              hipStream_t stream) {
  const int K = 576, N = 4608;
  int M = in_sizes[0] / K;
  int waves = (M / 16) * (N / 16);
  k_naive<<<waves / 4, 256, 0, stream>>>((const unsigned short*)d_in[0], (const int*)d_in[1], (const unsigned short*)d_in[2],
                                  (__hip_bfloat16*)d_out, M, K, N);
}
```

```cpp
#include <hip/hip_runtime.h>
#include <hip/hip_bf16.h>

__global__ __launch_bounds__(512, 2) void sqllm_gemm_kernel(const void* x, unsigned xb, unsigned xf,
                                                          const void* qw, unsigned qb, unsigned qf,
                                                          const void* lut, unsigned lb, unsigned lf,
                                                          void* out, unsigned ob, unsigned of) {
  __shared__ __attribute__((aligned(1024))) char lds[163840];
  unsigned tid = threadIdx.x;
  asm volatile(
    "s_load_dwordx16 s[16:31], %1, 0x0\n"
    "v_readfirstlane_b32 s36, %0\n"
    "s_lshr_b32 s8, %2, 3\n"
    "s_and_b32 s78, %2, 1\n"
    "s_lshl_b32 s79, s78, 5\n"
    "s_add_u32 s78, s78, s8\n"
    "s_cmp_ge_u32 s78, 32\n"
    "s_cbranch_scc1 L_end_%=\n"
    "s_bfe_u32 s78, %2, 0x20001\n"
    "s_mul_i32 s78, s78, 63\n"
    "s_add_u32 s79, s79, s8\n"
    "s_add_u32 s77, s78, s79\n"
    "s_mul_i32 s9, s77, 586\n"
    "s_lshr_b32 s9, s9, 12\n"
    "v_and_b32_e32 v4, 63, %0\n"
    "s_lshr_b32 s36, s36, 6\n"
    "s_and_b32 s37, s36, 3\n"
    "s_mul_i32 s10, s9, 7\n"
    "s_sub_u32 s10, s77, s10\n"
    "s_cmp_lt_u32 s36, 4\n"
    "s_cbranch_scc1 L_M_%=\n"
    "v_lshrrev_b32_e64 v6, 5, v4\n"
    "v_mul_u32_u24_e32 v2, 18432, v6\n"
    "s_and_b32 s81, s37, 1\n"
    "s_lshl_b32 s79, s81, 3\n"
    "v_and_b32_e32 v6, 31, v4\n"
    "v_xor_b32_e32 v6, s79, v6\n"
    "s_mul_i32 s78, s37, 147456\n"
    "s_lshl_b32 s79, s9, 9\n"
    "s_add_u32 s78, s78, s79\n"
    "v_lshl_add_u32 v2, v6, 4, v2\n"
    "v_add_u32_e64 v2, s78, v2\n"
    "s_mov_b32 s44, 36864\n"
    "s_add_u32 s45, s44, s44\n"
    "s_add_u32 s46, s45, s44\n"
    "s_lshl_b32 s47, s44, 5\n"
    "s_lshl_b32 s48, s37, 12\n"
    "s_min_u32 s80, s10, 2\n"
    "s_mul_i32 s11, s10, 18\n"
    "s_add_u32 s11, s11, s80\n"
    "s_lshl_b32 s11, s11, 4\n"
    "s_lshl_b32 s38, s37, 10\n"
    "v_lshrrev_b32_e32 v6, 3, v4\n"
    "v_mul_u32_u24_e32 v3, 1152, v6\n"
    "s_lshl_b32 s79, s81, 2\n"
    "v_lshrrev_b32_e32 v7, 4, v4\n"
    "v_xor_b32_e32 v6, v4, v7\n"
    "v_xor_b32_e32 v6, s79, v6\n"
    "v_and_b32_e32 v6, 7, v6\n"
    "s_lshl_b32 s78, s81, 3\n"
    "s_add_u32 s78, s78, s11\n"
    "s_mul_i32 s78, s78, 1152\n"
    "v_lshl_add_u32 v3, v6, 4, v3\n"
    "v_add_u32_e32 v3, s78, v3\n"
    "s_lshr_b32 s81, s37, 1\n"
    "s_mul_i32 s39, s81, 18432\n"
    "s_add_u32 s40, s39, s44\n"
    "s_add_u32 s41, s40, s44\n"
    "s_add_u32 s42, s41, s44\n"
    "s_waitcnt lgkmcnt(0)\n"
    "s_add_u32 m0, s48, 0\n"
    "s_nop 0\n"
    "buffer_load_dwordx4 v2, s[20:23], 0 offen lds\n"
    "s_add_u32 m0, s48, 1024\n"
    "s_nop 0\n"
    "buffer_load_dwordx4 v2, s[20:23], s44 offen lds\n"
    "s_add_u32 m0, s48, 2048\n"
    "s_nop 0\n"
    "buffer_load_dwordx4 v2, s[20:23], s45 offen lds\n"
    "s_add_u32 m0, s48, 3072\n"
    "s_nop 0\n"
    "buffer_load_dwordx4 v2, s[20:23], s46 offen lds\n"
    "v_add_u32_e32 v1, 589824, v2\n"
    "s_add_u32 m0, s48, 16384\n"
    "s_nop 0\n"
    "buffer_load_dwordx4 v1, s[20:23], 0 offen lds\n"
    "s_add_u32 m0, s48, 17408\n"
    "s_nop 0\n"
    "buffer_load_dwordx4 v1, s[20:23], s44 offen lds\n"
    "s_add_u32 m0, s48, 18432\n"
    "s_nop 0\n"
    "buffer_load_dwordx4 v1, s[20:23], s45 offen lds\n"
    "s_add_u32 m0, s48, 19456\n"
    "s_nop 0\n"
    "buffer_load_dwordx4 v1, s[20:23], s46 offen lds\n"
    "s_add_u32 m0, s38, 114688\n"
    "s_nop 0\n"
    "buffer_load_dwordx4 v3, s[16:19], s39 offen lds\n"
    "s_add_u32 m0, s38, 118784\n"
    "s_nop 0\n"
    "buffer_load_dwordx4 v3, s[16:19], s40 offen lds\n"
    "s_add_u32 m0, s38, 122880\n"
    "s_nop 0\n"
    "buffer_load_dwordx4 v3, s[16:19], s41 offen lds\n"
    "s_add_u32 m0, s38, 126976\n"
    "s_nop 0\n"
    "buffer_load_dwordx4 v3, s[16:19], s42 offen lds\n"
    "s_branch L_Lbody_%=\n"
    "L_M_%=:\n"
    "s_getpc_b64 s[82:83]\n"
    "L_pc_%=:\n"
    "s_and_b32 s78, s8, 7\n"
    "s_lshl_b32 s78, s78, 2\n"
    "s_add_u32 s78, s78, s37\n"
    "s_lshl_b32 s78, s78, 10\n"
    "v_min_u32_e32 v205, 7, v4\n"
    "v_lshlrev_b32_e32 v205, 7, v205\n"
    "v_add_u32_e64 v205, s78, v205\n"
    "v_min_u32_e32 v205, L_end_%=-L_pc_%=, v205\n"
    "global_load_dword v205, v205, s[82:83]\n"
    "s_min_u32 s80, s10, 2\n"
    "s_mul_i32 s11, s10, 18\n"
    "s_add_u32 s11, s11, s80\n"
    "s_lshl_b32 s11, s11, 4\n"
    "s_lshl_b32 s38, s37, 10\n"
    "v_lshrrev_b32_e32 v204, 2, v4\n"
    "v_mul_u32_u24_e32 v202, 9216, v204\n"
    "v_and_b32_e64 v204, 3, v4\n"
    "v_lshl_add_u32 v202, v204, 4, v202\n"
    "s_lshl_b32 s51, s9, 2\n"
    "s_add_u32 s51, s51, s37\n"
    "s_lshl_b32 s77, s51, 6\n"
    "s_mul_i32 s78, s51, 8\n"
    "s_mul_i32 s79, s11, 9\n"
    "s_add_u32 s78, s78, s79\n"
    "v_min_u32_e32 v206, 7, v4\n"
    "v_add_u32_e32 v206, s78, v206\n"
    "v_lshlrev_b32_e64 v206, 7, v206\n"
    "s_add_u32 m0, s38, 106496\n"
    "s_waitcnt lgkmcnt(0)\n"
    "buffer_load_dwordx4 v202, s[24:27], s77 offen lds\n"
    "buffer_load_dword v206, v206, s[16:19], 0 offen\n"
    "s_setprio 2\n"
    "v_lshrrev_b32_e32 v200, 4, v4\n"
    "v_and_b32_e32 v201, 15, v4\n"
    "s_add_u32 s78, s38, 106496\n"
    "v_lshl_add_u32 v202, v201, 2, v200\n"
    "v_add_u32_e32 v202, s78, v202\n"
    "v_lshlrev_b32_e32 v203, 4, v4\n"
    "v_add_u32_e32 v203, s78, v203\n"
    "v_lshlrev_b32_e32 v204, 4, v201\n"
    "v_add_u32_e32 v204, s78, v204\n"
    "v_and_b32_e32 v5, 1, v200\n"
    "v_xor_b32_e32 v5, s37, v5\n"
    "v_lshlrev_b32_e32 v5, 7, v5\n"
    "v_lshl_add_u32 v5, v200, 12, v5\n"
    "v_lshl_add_u32 v5, v201, 3, v5\n"
    "v_lshrrev_b32_e32 v1, 1, v201\n"
    "v_xor_b32_e32 v1, v1, v200\n"
    "v_lshlrev_b32_e64 v1, 4, v1\n"
    "v_lshl_add_u32 v1, v201, 7, v1\n"
    "v_add_u32_e32 v1, 114688, v1\n"
    "s_mul_i32 s79, s11, 9216\n"
    "s_add_u32 s51, s77, s79\n"
    "s_mov_b32 s74, 0x8080808\n"
    "s_mov_b32 s75, 0x5010400\n"
    "s_mov_b32 s76, 0x7030602\n"
    "s_mov_b32 s49, 0xb0a0908\n"
    "s_mov_b32 s50, 0xffffff00\n"
    "s_waitcnt vmcnt(1)\n"
    "ds_read_u8 v48, v202 offset:0\n"
    "ds_read_u8 v49, v202 offset:64\n"
    "ds_read_u8 v50, v202 offset:128\n"
    "ds_read_u8 v51, v202 offset:192\n"
    "ds_read_u8 v52, v202 offset:256\n"
    "ds_read_u8 v53, v202 offset:320\n"
    "ds_read_u8 v54, v202 offset:384\n"
    "ds_read_u8 v55, v202 offset:448\n"
    "ds_read_u8 v56, v202 offset:512\n"
    "ds_read_u8 v57, v202 offset:576\n"
    "ds_read_u8 v58, v202 offset:640\n"
    "ds_read_u8 v59, v202 offset:704\n"
    "ds_read_u8 v60, v202 offset:768\n"
    "ds_read_u8 v61, v202 offset:832\n"
    "ds_read_u8 v62, v202 offset:896\n"
    "s_waitcnt lgkmcnt(14)\n"
    "ds_read_u8 v63, v202 offset:960\n"
    "s_waitcnt lgkmcnt(12)\n"
    "v_lshl_or_b32 v48, v49, 8, v48\n"
    "v_lshl_or_b32 v50, v51, 8, v50\n"
    "v_lshl_or_b32 v64, v50, 16, v48\n"
    "s_waitcnt lgkmcnt(8)\n"
    "v_lshl_or_b32 v52, v53, 8, v52\n"
    "v_lshl_or_b32 v54, v55, 8, v54\n"
    "v_lshl_or_b32 v65, v54, 16, v52\n"
    "s_waitcnt lgkmcnt(4)\n"
    "v_lshl_or_b32 v56, v57, 8, v56\n"
    "v_lshl_or_b32 v58, v59, 8, v58\n"
    "v_lshl_or_b32 v66, v58, 16, v56\n"
    "s_waitcnt lgkmcnt(0)\n"
    "v_lshl_or_b32 v60, v61, 8, v60\n"
    "v_lshl_or_b32 v62, v63, 8, v62\n"
    "v_lshl_or_b32 v67, v62, 16, v60\n"
    "v_xor_b32_e32 v69, s50, v65\n"
    "v_xor_b32_e64 v71, s50, v67\n"
    "v_perm_b32 v48, v71, v66, s49\n"
    "v_perm_b32 v49, v69, v64, s49\n"
    "v_xor_b32_e32 v68, v64, v48\n"
    "v_xor_b32_e32 v70, v66, v49\n"
    "v_perm_b32 v48, v71, v70, s49\n"
    "v_perm_b32 v49, v69, v68, s49\n"
    "v_xor_b32_e32 v68, v64, v48\n"
    "v_xor_b32_e32 v70, v66, v49\n"
    "v_perm_b32 v48, v71, v70, s49\n"
    "v_perm_b32 v49, v69, v68, s49\n"
    "v_xor_b32_e32 v68, v64, v48\n"
    "v_xor_b32_e32 v70, v66, v49\n"
    "v_mov_b32_e32 v16, v68\n"
    "v_mov_b32_e32 v20, v68\n"
    "v_mov_b32_e32 v17, v69\n"
    "v_mov_b32_e32 v21, v69\n"
    "v_mov_b32_e32 v18, v70\n"
    "v_mov_b32_e32 v22, v70\n"
    "v_mov_b32_e32 v19, v71\n"
    "v_mov_b32_e32 v23, v71\n"
    "v_permlane16_swap_b32_e32 v16, v20\n"
    "v_permlane16_swap_b32_e32 v17, v21\n"
    "v_permlane16_swap_b32_e32 v18, v22\n"
    "v_permlane16_swap_b32_e32 v19, v23\n"
    "v_mov_b32_e32 v24, v16\n"
    "v_mov_b32_e32 v25, v17\n"
    "v_mov_b32_e32 v26, v18\n"
    "v_mov_b32_e32 v27, v19\n"
    "v_mov_b32_e32 v28, v20\n"
    "v_mov_b32_e32 v29, v21\n"
    "v_mov_b32_e32 v30, v22\n"
    "v_mov_b32_e32 v31, v23\n"
    "v_permlane32_swap_b32_e32 v16, v24\n"
    "v_permlane32_swap_b32_e32 v17, v25\n"
    "v_permlane32_swap_b32_e32 v18, v26\n"
    "v_permlane32_swap_b32_e32 v19, v27\n"
    "v_permlane32_swap_b32_e32 v20, v28\n"
    "v_permlane32_swap_b32_e32 v21, v29\n"
    "v_permlane32_swap_b32_e32 v22, v30\n"
    "v_permlane32_swap_b32_e32 v23, v31\n"
    "s_branch L_Mbody_%=\n"
    "L_Lbody_%=:\n"
    "s_mov_b32 s14, 0x87654321\n"
    "s_mov_b32 s15, s14\n"
    "s_lshl_b32 s80, s10, 2\n"
    "s_lshr_b64 s[14:15], s[14:15], s80\n"
    "s_lshl_b64 s[14:15], s[14:15], 4\n"
    "s_bfe_u32 s80, s14, 0x40004\n"
    "s_mul_i32 s80, s80, s47\n"
    "v_add_u32_e64 v1, s80, v2\n"
    "s_add_u32 m0, s48, 32768\n"
    "s_nop 0\n"
    "buffer_load_dwordx4 v1, s[20:23], 0 offen lds\n"
    "s_add_u32 m0, s48, 33792\n"
    "s_nop 0\n"
    "buffer_load_dwordx4 v1, s[20:23], s44 offen lds\n"
    "s_add_u32 m0, s48, 34816\n"
    "s_nop 0\n"
    "buffer_load_dwordx4 v1, s[20:23], s45 offen lds\n"
    "s_add_u32 m0, s48, 35840\n"
    "s_nop 0\n"
    "buffer_load_dwordx4 v1, s[20:23], s46 offen lds\n"
    "s_lshl_b32 s78, s9, 2\n"
    "s_add_u32 s78, s78, s37\n"
    "s_mul_i32 s78, s78, 11\n"
    "s_mul_i32 s79, s11, 9\n"
    "s_add_u32 s78, s78, s79\n"
    "s_add_u32 s78, s78, 1152\n"
    "v_min_u32_e32 v8, 10, v4\n"
    "v_add_u32_e32 v8, s78, v8\n"
    "v_lshlrev_b32_e32 v8, 7, v8\n"
    "s_bfe_u32 s49, s14, 0x40000\n"
    "s_lshl_b32 s49, s49, 7\n"
    "s_bfe_u32 s50, s14, 0x40004\n"
    "s_lshl_b32 s50, s50, 7\n"
    "s_bfe_u32 s51, s14, 0x40008\n"
    "s_lshl_b32 s51, s51, 7\n"
    "s_bfe_u32 s52, s14, 0x4000c\n"
    "s_lshl_b32 s52, s52, 7\n"
    "s_bfe_u32 s53, s14, 0x40010\n"
    "s_lshl_b32 s53, s53, 7\n"
    "s_bfe_u32 s54, s14, 0x40014\n"
    "s_lshl_b32 s54, s54, 7\n"
    "s_bfe_u32 s55, s14, 0x40018\n"
    "s_lshl_b32 s55, s55, 7\n"
    "s_bfe_u32 s56, s14, 0x4001c\n"
    "s_lshl_b32 s56, s56, 7\n"
    "s_and_b32 s57, s15, 15\n"
    "s_lshl_b32 s57, s57, 7\n"
    "s_mov_b32 s77, s49\n"
    "s_cmp_eq_u32 s81, 0\n"
    "s_cselect_b32 s49, s49, s50\n"
    "s_cselect_b32 s50, s50, s51\n"
    "s_cselect_b32 s51, s51, s52\n"
    "s_cselect_b32 s52, s52, s53\n"
    "s_cselect_b32 s53, s53, s54\n"
    "s_cselect_b32 s54, s54, s55\n"
    "s_cselect_b32 s55, s55, s56\n"
    "s_cselect_b32 s56, s56, s57\n"
    "s_cselect_b32 s57, s57, s77\n"
    "s_cmp_lt_u32 s10, 2\n"
    "s_cselect_b32 s13, 11, 10\n"
    "s_mul_i32 s80, s9, 52\n"
    "s_lshr_b32 s80, s80, 8\n"
    "s_mul_i32 s80, s80, 5\n"
    "s_sub_u32 s12, s9, s80\n"
    "s_mul_i32 s12, s12, 8\n"
    "s_lshr_b32 s12, s12, 2\n"
    "s_movk_i32 s78, 18432\n"
    "s_add_u32 s58, s12, 0\n"
    "s_sub_u32 s80, s58, s13\n"
    "s_min_u32 s58, s58, s80\n"
    "s_add_u32 s58, s58, 8\n"
    "s_mul_i32 s58, s58, s78\n"
    "s_add_u32 s59, s12, 1\n"
    "s_sub_u32 s80, s59, s13\n"
    "s_min_u32 s59, s59, s80\n"
    "s_add_u32 s59, s59, 8\n"
    "s_mul_i32 s59, s59, s78\n"
    "s_add_u32 s60, s12, 2\n"
    "s_sub_u32 s80, s60, s13\n"
    "s_min_u32 s60, s60, s80\n"
    "s_add_u32 s60, s60, 8\n"
    "s_mul_i32 s60, s60, s78\n"
    "s_add_u32 s61, s12, 3\n"
    "s_sub_u32 s80, s61, s13\n"
    "s_min_u32 s61, s61, s80\n"
    "s_add_u32 s61, s61, 8\n"
    "s_mul_i32 s61, s61, s78\n"
    "s_add_u32 s62, s12, 4\n"
    "s_sub_u32 s80, s62, s13\n"
    "s_min_u32 s62, s62, s80\n"
    "s_add_u32 s62, s62, 8\n"
    "s_mul_i32 s62, s62, s78\n"
    "s_add_u32 s63, s12, 5\n"
    "s_sub_u32 s80, s63, s13\n"
    "s_min_u32 s63, s63, s80\n"
    "s_add_u32 s63, s63, 8\n"
    "s_mul_i32 s63, s63, s78\n"
    "s_add_u32 s64, s12, 6\n"
    "s_sub_u32 s80, s64, s13\n"
    "s_min_u32 s64, s64, s80\n"
    "s_add_u32 s64, s64, 8\n"
    "s_mul_i32 s64, s64, s78\n"
    "s_add_u32 s65, s12, 7\n"
    "s_sub_u32 s80, s65, s13\n"
    "s_min_u32 s65, s65, s80\n"
    "s_add_u32 s65, s65, 8\n"
    "s_mul_i32 s65, s65, s78\n"
    "s_add_u32 s66, s12, 8\n"
    "s_sub_u32 s80, s66, s13\n"
    "s_min_u32 s66, s66, s80\n"
    "s_add_u32 s66, s66, 8\n"
    "s_mul_i32 s66, s66, s78\n"
    "s_add_u32 s67, s12, 9\n"
    "s_sub_u32 s80, s67, s13\n"
    "s_min_u32 s67, s67, s80\n"
    "s_add_u32 s67, s67, 8\n"
    "s_mul_i32 s67, s67, s78\n"
    "s_add_u32 s68, s12, 10\n"
    "s_sub_u32 s80, s68, s13\n"
    "s_min_u32 s68, s68, s80\n"
    "s_add_u32 s68, s68, 8\n"
    "s_mul_i32 s68, s68, s78\n"
    "s_mov_b32 s69, s68\n"
    "s_waitcnt vmcnt(12)\n"
    "s_barrier\n"
    "s_bfe_u32 s80, s14, 0x40004\n"
    "s_lshl_b32 s80, s80, 7\n"
    "v_add_u32_e32 v5, s80, v3\n"
    "s_add_u32 m0, s38, 131072\n"
    "s_nop 0\n"
    "buffer_load_dwordx4 v5, s[16:19], s39 offen lds\n"
    "s_add_u32 m0, s38, 135168\n"
    "s_nop 0\n"
    "buffer_load_dwordx4 v5, s[16:19], s40 offen lds\n"
    "v_add_u32_e32 v1, 589824, v1\n"
    "s_add_u32 m0, s48, 49152\n"
    "s_nop 0\n"
    "buffer_load_dwordx4 v1, s[20:23], 0 offen lds\n"
    "s_add_u32 m0, s48, 50176\n"
    "s_nop 0\n"
    "buffer_load_dwordx4 v1, s[20:23], s44 offen lds\n"
    "s_add_u32 m0, s48, 51200\n"
    "s_nop 0\n"
    "buffer_load_dwordx4 v1, s[20:23], s45 offen lds\n"
    "s_add_u32 m0, s48, 52224\n"
    "s_nop 0\n"
    "buffer_load_dwordx4 v1, s[20:23], s46 offen lds\n"
    "s_waitcnt vmcnt(10)\n"
    "s_barrier\n"
    "s_add_u32 m0, s38, 139264\n"
    "s_nop 0\n"
    "buffer_load_dwordx4 v5, s[16:19], s41 offen lds\n"
    "s_add_u32 m0, s38, 143360\n"
    "s_nop 0\n"
    "buffer_load_dwordx4 v5, s[16:19], s42 offen lds\n"
    "s_bfe_u32 s80, s14, 0x40008\n"
    "s_mul_i32 s80, s80, s47\n"
    "v_add_u32_e32 v1, s80, v2\n"
    "s_add_u32 m0, s48, 0\n"
    "s_nop 0\n"
    "buffer_load_dwordx4 v1, s[20:23], 0 offen lds\n"
    "s_add_u32 m0, s48, 1024\n"
    "s_nop 0\n"
    "buffer_load_dwordx4 v1, s[20:23], s44 offen lds\n"
    "s_add_u32 m0, s48, 2048\n"
    "s_nop 0\n"
    "buffer_load_dwordx4 v1, s[20:23], s45 offen lds\n"
    "s_add_u32 m0, s48, 3072\n"
    "s_nop 0\n"
    "buffer_load_dwordx4 v1, s[20:23], s46 offen lds\n"
    "s_waitcnt vmcnt(12)\n"
    "s_barrier\n"
    "s_bfe_u32 s80, s14, 0x40008\n"
    "s_lshl_b32 s80, s80, 7\n"
    "v_add_u32_e64 v5, s80, v3\n"
    "s_add_u32 m0, s38, 147456\n"
    "s_nop 0\n"
    "buffer_load_dwordx4 v5, s[16:19], s39 offen lds\n"
    "s_add_u32 m0, s38, 151552\n"
    "s_nop 0\n"
    "buffer_load_dwordx4 v5, s[16:19], s40 offen lds\n"
    "v_add_u32_e32 v1, 589824, v1\n"
    "s_add_u32 m0, s48, 16384\n"
    "s_nop 0\n"
    "buffer_load_dwordx4 v1, s[20:23], 0 offen lds\n"
    "s_add_u32 m0, s48, 17408\n"
    "s_nop 0\n"
    "buffer_load_dwordx4 v1, s[20:23], s44 offen lds\n"
    "s_add_u32 m0, s48, 18432\n"
    "s_nop 0\n"
    "buffer_load_dwordx4 v1, s[20:23], s45 offen lds\n"
    "s_add_u32 m0, s48, 19456\n"
    "s_nop 0\n"
    "buffer_load_dwordx4 v1, s[20:23], s46 offen lds\n"
    "s_waitcnt vmcnt(10)\n"
    "s_barrier\n"
    "s_add_u32 m0, s38, 155648\n"
    "s_nop 0\n"
    "buffer_load_dwordx4 v5, s[16:19], s41 offen lds\n"
    "s_add_u32 m0, s38, 159744\n"
    "s_nop 0\n"
    "buffer_load_dwordx4 v5, s[16:19], s42 offen lds\n"
    "s_bfe_u32 s80, s14, 0x4000c\n"
    "s_mul_i32 s80, s80, s47\n"
    "v_add_u32_e32 v1, s80, v2\n"
    "s_add_u32 m0, s48, 32768\n"
    "s_nop 0\n"
    "buffer_load_dwordx4 v1, s[20:23], 0 offen lds\n"
    "s_add_u32 m0, s48, 33792\n"
    "s_nop 0\n"
    "buffer_load_dwordx4 v1, s[20:23], s44 offen lds\n"
    "s_add_u32 m0, s48, 34816\n"
    "s_nop 0\n"
    "buffer_load_dwordx4 v1, s[20:23], s45 offen lds\n"
    "s_add_u32 m0, s48, 35840\n"
    "s_nop 0\n"
    "buffer_load_dwordx4 v1, s[20:23], s46 offen lds\n"
    "s_waitcnt vmcnt(12)\n"
    "s_barrier\n"
    "s_bfe_u32 s80, s14, 0x4000c\n"
    "s_lshl_b32 s80, s80, 7\n"
    "v_add_u32_e32 v5, s80, v3\n"
    "s_add_u32 m0, s38, 114688\n"
    "s_nop 0\n"
    "buffer_load_dwordx4 v5, s[16:19], s39 offen lds\n"
    "s_add_u32 m0, s38, 118784\n"
    "s_nop 0\n"
    "buffer_load_dwordx4 v5, s[16:19], s40 offen lds\n"
    "v_add_u32_e32 v1, 589824, v1\n"
    "s_add_u32 m0, s48, 49152\n"
    "s_nop 0\n"
    "buffer_load_dwordx4 v1, s[20:23], 0 offen lds\n"
    "s_add_u32 m0, s48, 50176\n"
    "s_nop 0\n"
    "buffer_load_dwordx4 v1, s[20:23], s44 offen lds\n"
    "s_add_u32 m0, s48, 51200\n"
    "s_nop 0\n"
    "buffer_load_dwordx4 v1, s[20:23], s45 offen lds\n"
    "s_add_u32 m0, s48, 52224\n"
    "s_nop 0\n"
    "buffer_load_dwordx4 v1, s[20:23], s46 offen lds\n"
    "s_waitcnt vmcnt(10)\n"
    "s_barrier\n"
    "s_add_u32 m0, s38, 122880\n"
    "s_nop 0\n"
    "buffer_load_dwordx4 v5, s[16:19], s41 offen lds\n"
    "s_add_u32 m0, s38, 126976\n"
    "s_nop 0\n"
    "buffer_load_dwordx4 v5, s[16:19], s42 offen lds\n"
    "s_bfe_u32 s80, s14, 0x40010\n"
    "s_mul_i32 s80, s80, s47\n"
    "v_add_u32_e32 v1, s80, v2\n"
    "s_add_u32 m0, s48, 0\n"
    "s_nop 0\n"
    "buffer_load_dwordx4 v1, s[20:23], 0 offen lds\n"
    "s_add_u32 m0, s48, 1024\n"
    "s_nop 0\n"
    "buffer_load_dwordx4 v1, s[20:23], s44 offen lds\n"
    "s_add_u32 m0, s48, 2048\n"
    "s_nop 0\n"
    "buffer_load_dwordx4 v1, s[20:23], s45 offen lds\n"
    "s_add_u32 m0, s48, 3072\n"
    "s_nop 0\n"
    "buffer_load_dwordx4 v1, s[20:23], s46 offen lds\n"
    "s_waitcnt vmcnt(12)\n"
    "s_barrier\n"
    "s_bfe_u32 s80, s14, 0x40010\n"
    "s_lshl_b32 s80, s80, 7\n"
    "v_add_u32_e64 v5, s80, v3\n"
    "s_add_u32 m0, s38, 131072\n"
    "s_nop 0\n"
    "buffer_load_dwordx4 v5, s[16:19], s39 offen lds\n"
    "s_add_u32 m0, s38, 135168\n"
    "s_nop 0\n"
    "buffer_load_dwordx4 v5, s[16:19], s40 offen lds\n"
    "v_add_u32_e32 v1, 589824, v1\n"
    "s_add_u32 m0, s48, 16384\n"
    "s_nop 0\n"
    "buffer_load_dwordx4 v1, s[20:23], 0 offen lds\n"
    "s_add_u32 m0, s48, 17408\n"
    "s_nop 0\n"
    "buffer_load_dwordx4 v1, s[20:23], s44 offen lds\n"
    "s_add_u32 m0, s48, 18432\n"
    "s_nop 0\n"
    "buffer_load_dwordx4 v1, s[20:23], s45 offen lds\n"
    "s_add_u32 m0, s48, 19456\n"
    "s_nop 0\n"
    "buffer_load_dwordx4 v1, s[20:23], s46 offen lds\n"
    "buffer_load_dword v8, v8, s[16:19], 0 offen\n"
    "s_waitcnt vmcnt(11)\n"
    "s_barrier\n"
    "s_add_u32 m0, s38, 139264\n"
    "s_nop 0\n"
    "buffer_load_dwordx4 v5, s[16:19], s41 offen lds\n"
    "s_add_u32 m0, s38, 143360\n"
    "s_nop 0\n"
    "buffer_load_dwordx4 v5, s[16:19], s42 offen lds\n"
    "s_bfe_u32 s80, s14, 0x40014\n"
    "s_mul_i32 s80, s80, s47\n"
    "v_add_u32_e32 v1, s80, v2\n"
    "s_add_u32 m0, s48, 32768\n"
    "s_nop 0\n"
    "buffer_load_dwordx4 v1, s[20:23], 0 offen lds\n"
    "s_add_u32 m0, s48, 33792\n"
    "s_nop 0\n"
    "buffer_load_dwordx4 v1, s[20:23], s44 offen lds\n"
    "s_add_u32 m0, s48, 34816\n"
    "s_nop 0\n"
    "buffer_load_dwordx4 v1, s[20:23], s45 offen lds\n"
    "s_add_u32 m0, s48, 35840\n"
    "s_nop 0\n"
    "buffer_load_dwordx4 v1, s[20:23], s46 offen lds\n"
    "s_waitcnt vmcnt(13)\n"
    "s_barrier\n"
    "s_bfe_u32 s80, s14, 0x40014\n"
    "s_lshl_b32 s80, s80, 7\n"
    "v_add_u32_e32 v5, s80, v3\n"
    "s_add_u32 m0, s38, 147456\n"
    "s_nop 0\n"
    "buffer_load_dwordx4 v5, s[16:19], s39 offen lds\n"
    "s_add_u32 m0, s38, 151552\n"
    "s_nop 0\n"
    "buffer_load_dwordx4 v5, s[16:19], s40 offen lds\n"
    "v_add_u32_e32 v1, 589824, v1\n"
    "s_add_u32 m0, s48, 49152\n"
    "s_nop 0\n"
    "buffer_load_dwordx4 v1, s[20:23], 0 offen lds\n"
    "s_add_u32 m0, s48, 50176\n"
    "s_nop 0\n"
    "buffer_load_dwordx4 v1, s[20:23], s44 offen lds\n"
    "s_add_u32 m0, s48, 51200\n"
    "s_nop 0\n"
    "buffer_load_dwordx4 v1, s[20:23], s45 offen lds\n"
    "s_add_u32 m0, s48, 52224\n"
    "s_nop 0\n"
    "buffer_load_dwordx4 v1, s[20:23], s46 offen lds\n"
    "s_waitcnt vmcnt(10)\n"
    "s_barrier\n"
    "s_add_u32 m0, s38, 155648\n"
    "s_nop 0\n"
    "buffer_load_dwordx4 v5, s[16:19], s41 offen lds\n"
    "s_add_u32 m0, s38, 159744\n"
    "s_nop 0\n"
    "buffer_load_dwordx4 v5, s[16:19], s42 offen lds\n"
    "s_bfe_u32 s80, s14, 0x40018\n"
    "s_mul_i32 s80, s80, s47\n"
    "v_add_u32_e32 v1, s80, v2\n"
    "s_add_u32 m0, s48, 0\n"
    "s_nop 0\n"
    "buffer_load_dwordx4 v1, s[20:23], 0 offen lds\n"
    "s_add_u32 m0, s48, 1024\n"
    "s_nop 0\n"
    "buffer_load_dwordx4 v1, s[20:23], s44 offen lds\n"
    "s_add_u32 m0, s48, 2048\n"
    "s_nop 0\n"
    "buffer_load_dwordx4 v1, s[20:23], s45 offen lds\n"
    "s_add_u32 m0, s48, 3072\n"
    "s_nop 0\n"
    "buffer_load_dwordx4 v1, s[20:23], s46 offen lds\n"
    "s_waitcnt vmcnt(12)\n"
    "s_barrier\n"
    "s_bfe_u32 s80, s14, 0x40018\n"
    "s_lshl_b32 s80, s80, 7\n"
    "v_add_u32_e64 v5, s80, v3\n"
    "s_add_u32 m0, s38, 114688\n"
    "s_nop 0\n"
    "buffer_load_dwordx4 v5, s[16:19], s39 offen lds\n"
    "s_add_u32 m0, s38, 118784\n"
    "s_nop 0\n"
    "buffer_load_dwordx4 v5, s[16:19], s40 offen lds\n"
    "v_add_u32_e32 v1, 589824, v1\n"
    "s_add_u32 m0, s48, 16384\n"
    "s_nop 0\n"
    "buffer_load_dwordx4 v1, s[20:23], 0 offen lds\n"
    "s_add_u32 m0, s48, 17408\n"
    "s_nop 0\n"
    "buffer_load_dwordx4 v1, s[20:23], s44 offen lds\n"
    "s_add_u32 m0, s48, 18432\n"
    "s_nop 0\n"
    "buffer_load_dwordx4 v1, s[20:23], s45 offen lds\n"
    "s_add_u32 m0, s48, 19456\n"
    "s_nop 0\n"
    "buffer_load_dwordx4 v1, s[20:23], s46 offen lds\n"
    "s_waitcnt vmcnt(10)\n"
    "s_barrier\n"
    "s_add_u32 m0, s38, 122880\n"
    "s_nop 0\n"
    "buffer_load_dwordx4 v5, s[16:19], s41 offen lds\n"
    "s_add_u32 m0, s38, 126976\n"
    "s_nop 0\n"
    "buffer_load_dwordx4 v5, s[16:19], s42 offen lds\n"
    "s_bfe_u32 s80, s14, 0x4001c\n"
    "s_mul_i32 s80, s80, s47\n"
    "v_add_u32_e32 v1, s80, v2\n"
    "s_add_u32 m0, s48, 32768\n"
    "s_nop 0\n"
    "buffer_load_dwordx4 v1, s[20:23], 0 offen lds\n"
    "s_add_u32 m0, s48, 33792\n"
    "s_nop 0\n"
    "buffer_load_dwordx4 v1, s[20:23], s44 offen lds\n"
    "s_add_u32 m0, s48, 34816\n"
    "s_nop 0\n"
    "buffer_load_dwordx4 v1, s[20:23], s45 offen lds\n"
    "s_add_u32 m0, s48, 35840\n"
    "s_nop 0\n"
    "buffer_load_dwordx4 v1, s[20:23], s46 offen lds\n"
    "s_waitcnt vmcnt(12)\n"
    "s_barrier\n"
    "s_bfe_u32 s80, s14, 0x4001c\n"
    "s_lshl_b32 s80, s80, 7\n"
    "v_add_u32_e32 v5, s80, v3\n"
    "s_add_u32 m0, s38, 131072\n"
    "s_nop 0\n"
    "buffer_load_dwordx4 v5, s[16:19], s39 offen lds\n"
    "s_add_u32 m0, s38, 135168\n"
    "s_nop 0\n"
    "buffer_load_dwordx4 v5, s[16:19], s40 offen lds\n"
    "v_add_u32_e32 v1, 589824, v1\n"
    "s_add_u32 m0, s48, 49152\n"
    "s_nop 0\n"
    "buffer_load_dwordx4 v1, s[20:23], 0 offen lds\n"
    "s_add_u32 m0, s48, 50176\n"
    "s_nop 0\n"
    "buffer_load_dwordx4 v1, s[20:23], s44 offen lds\n"
    "s_add_u32 m0, s48, 51200\n"
    "s_nop 0\n"
    "buffer_load_dwordx4 v1, s[20:23], s45 offen lds\n"
    "s_add_u32 m0, s48, 52224\n"
    "s_nop 0\n"
    "buffer_load_dwordx4 v1, s[20:23], s46 offen lds\n"
    "s_waitcnt vmcnt(10)\n"
    "s_barrier\n"
    "s_add_u32 m0, s38, 139264\n"
    "s_nop 0\n"
    "buffer_load_dwordx4 v5, s[16:19], s41 offen lds\n"
    "s_add_u32 m0, s38, 143360\n"
    "s_nop 0\n"
    "buffer_load_dwordx4 v5, s[16:19], s42 offen lds\n"
    "s_and_b32 s80, s15, 15\n"
    "s_mul_i32 s80, s80, s47\n"
    "v_add_u32_e32 v1, s80, v2\n"
    "s_add_u32 m0, s48, 0\n"
    "s_nop 0\n"
    "buffer_load_dwordx4 v1, s[20:23], 0 offen lds\n"
    "s_add_u32 m0, s48, 1024\n"
    "s_nop 0\n"
    "buffer_load_dwordx4 v1, s[20:23], s44 offen lds\n"
    "s_add_u32 m0, s48, 2048\n"
    "s_nop 0\n"
    "buffer_load_dwordx4 v1, s[20:23], s45 offen lds\n"
    "s_add_u32 m0, s48, 3072\n"
    "s_nop 0\n"
    "buffer_load_dwordx4 v1, s[20:23], s46 offen lds\n"
    "s_waitcnt vmcnt(12)\n"
    "s_barrier\n"
    "s_and_b32 s80, s15, 15\n"
    "s_lshl_b32 s80, s80, 7\n"
    "v_add_u32_e64 v5, s80, v3\n"
    "s_add_u32 m0, s38, 147456\n"
    "s_nop 0\n"
    "buffer_load_dwordx4 v5, s[16:19], s39 offen lds\n"
    "s_add_u32 m0, s38, 151552\n"
    "s_nop 0\n"
    "buffer_load_dwordx4 v5, s[16:19], s40 offen lds\n"
    "v_add_u32_e32 v1, 589824, v1\n"
    "s_add_u32 m0, s48, 16384\n"
    "s_nop 0\n"
    "buffer_load_dwordx4 v1, s[20:23], 0 offen lds\n"
    "s_add_u32 m0, s48, 17408\n"
    "s_nop 0\n"
    "buffer_load_dwordx4 v1, s[20:23], s44 offen lds\n"
    "s_add_u32 m0, s48, 18432\n"
    "s_nop 0\n"
    "buffer_load_dwordx4 v1, s[20:23], s45 offen lds\n"
    "s_add_u32 m0, s48, 19456\n"
    "s_nop 0\n"
    "buffer_load_dwordx4 v1, s[20:23], s46 offen lds\n"
    "s_waitcnt vmcnt(10)\n"
    "s_barrier\n"
    "s_add_u32 m0, s38, 155648\n"
    "s_nop 0\n"
    "buffer_load_dwordx4 v5, s[16:19], s41 offen lds\n"
    "s_add_u32 m0, s38, 159744\n"
    "s_nop 0\n"
    "buffer_load_dwordx4 v5, s[16:19], s42 offen lds\n"
    "s_add_u32 m0, s38, 65536\n"
    "s_add_u32 s80, s58, s49\n"
    "buffer_load_dwordx4 v3, s[16:19], s80 offen lds\n"
    "s_add_u32 m0, s38, 69632\n"
    "s_add_u32 s80, s58, s51\n"
    "buffer_load_dwordx4 v3, s[16:19], s80 offen lds\n"
    "s_waitcnt vmcnt(10)\n"
    "s_barrier\n"
    "s_add_u32 m0, s38, 73728\n"
    "s_add_u32 s80, s58, s53\n"
    "buffer_load_dwordx4 v3, s[16:19], s80 offen lds\n"
    "s_add_u32 m0, s38, 77824\n"
    "s_add_u32 s80, s58, s55\n"
    "buffer_load_dwordx4 v3, s[16:19], s80 offen lds\n"
    "s_add_u32 m0, s38, 81920\n"
    "s_cmp_eq_u32 s81, 0\n"
    "s_cselect_b32 s80, s58, s59\n"
    "s_add_u32 s80, s80, s57\n"
    "buffer_load_dwordx4 v3, s[16:19], s80 offen lds\n"
    "s_waitcnt vmcnt(5)\n"
    "s_barrier\n"
    "s_add_u32 m0, s38, 86016\n"
    "s_add_u32 s80, s59, s50\n"
    "buffer_load_dwordx4 v3, s[16:19], s80 offen lds\n"
    "s_add_u32 m0, s38, 90112\n"
    "s_add_u32 s80, s59, s52\n"
    "buffer_load_dwordx4 v3, s[16:19], s80 offen lds\n"
    "s_add_u32 m0, s38, 94208\n"
    "s_add_u32 s80, s59, s54\n"
    "buffer_load_dwordx4 v3, s[16:19], s80 offen lds\n"
    "s_barrier\n"
    "s_add_u32 m0, s38, 98304\n"
    "s_add_u32 s80, s59, s56\n"
    "buffer_load_dwordx4 v3, s[16:19], s80 offen lds\n"
    "s_add_u32 m0, s38, 102400\n"
    "s_add_u32 s80, s60, s49\n"
    "buffer_load_dwordx4 v3, s[16:19], s80 offen lds\n"
    "s_add_u32 m0, s38, 106496\n"
    "s_add_u32 s80, s60, s51\n"
    "buffer_load_dwordx4 v3, s[16:19], s80 offen lds\n"
    "s_barrier\n"
    "s_add_u32 m0, s38, 110592\n"
    "s_add_u32 s80, s60, s53\n"
    "buffer_load_dwordx4 v3, s[16:19], s80 offen lds\n"
    "s_add_u32 m0, s38, 32768\n"
    "s_add_u32 s80, s60, s55\n"
    "buffer_load_dwordx4 v3, s[16:19], s80 offen lds\n"
    "s_add_u32 m0, s38, 36864\n"
    "s_cmp_eq_u32 s81, 0\n"
    "s_cselect_b32 s80, s60, s61\n"
    "s_add_u32 s80, s80, s57\n"
    "buffer_load_dwordx4 v3, s[16:19], s80 offen lds\n"
    "s_waitcnt vmcnt(9)\n"
    "s_barrier\n"
    "s_add_u32 m0, s38, 40960\n"
    "s_add_u32 s80, s61, s50\n"
    "buffer_load_dwordx4 v3, s[16:19], s80 offen lds\n"
    "s_add_u32 m0, s38, 45056\n"
    "s_add_u32 s80, s61, s52\n"
    "buffer_load_dwordx4 v3, s[16:19], s80 offen lds\n"
    "s_add_u32 m0, s38, 49152\n"
    "s_add_u32 s80, s61, s54\n"
    "buffer_load_dwordx4 v3, s[16:19], s80 offen lds\n"
    "s_add_u32 m0, s38, 53248\n"
    "s_add_u32 s80, s61, s56\n"
    "buffer_load_dwordx4 v3, s[16:19], s80 offen lds\n"
    "s_add_u32 m0, s38, 57344\n"
    "s_add_u32 s80, s62, s49\n"
    "buffer_load_dwordx4 v3, s[16:19], s80 offen lds\n"
    "s_waitcnt vmcnt(10)\n"
    "s_barrier\n"
    "s_add_u32 m0, s38, 61440\n"
    "s_add_u32 s80, s62, s51\n"
    "buffer_load_dwordx4 v3, s[16:19], s80 offen lds\n"
    "s_add_u32 m0, s38, 114688\n"
    "s_add_u32 s80, s62, s53\n"
    "buffer_load_dwordx4 v3, s[16:19], s80 offen lds\n"
    "s_add_u32 m0, s38, 118784\n"
    "s_add_u32 s80, s62, s55\n"
    "buffer_load_dwordx4 v3, s[16:19], s80 offen lds\n"
    "s_add_u32 m0, s38, 122880\n"
    "s_cmp_eq_u32 s81, 0\n"
    "s_cselect_b32 s80, s62, s63\n"
    "s_add_u32 s80, s80, s57\n"
    "buffer_load_dwordx4 v3, s[16:19], s80 offen lds\n"
    "s_add_u32 m0, s38, 126976\n"
    "s_add_u32 s80, s63, s50\n"
    "buffer_load_dwordx4 v3, s[16:19], s80 offen lds\n"
    "s_waitcnt vmcnt(10)\n"
    "s_barrier\n"
    "s_add_u32 m0, s38, 0\n"
    "s_add_u32 s80, s63, s52\n"
    "buffer_load_dwordx4 v3, s[16:19], s80 offen lds\n"
    "s_add_u32 m0, s38, 4096\n"
    "s_add_u32 s80, s63, s54\n"
    "buffer_load_dwordx4 v3, s[16:19], s80 offen lds\n"
    "s_add_u32 m0, s38, 8192\n"
    "s_add_u32 s80, s63, s56\n"
    "buffer_load_dwordx4 v3, s[16:19], s80 offen lds\n"
    "s_add_u32 m0, s38, 12288\n"
    "s_add_u32 s80, s64, s49\n"
    "buffer_load_dwordx4 v3, s[16:19], s80 offen lds\n"
    "s_add_u32 m0, s38, 16384\n"
    "s_add_u32 s80, s64, s51\n"
    "buffer_load_dwordx4 v3, s[16:19], s80 offen lds\n"
    "s_waitcnt vmcnt(11)\n"
    "s_barrier\n"
    "s_add_u32 m0, s38, 20480\n"
    "s_add_u32 s80, s64, s53\n"
    "buffer_load_dwordx4 v3, s[16:19], s80 offen lds\n"
    "s_add_u32 m0, s38, 24576\n"
    "s_add_u32 s80, s64, s55\n"
    "buffer_load_dwordx4 v3, s[16:19], s80 offen lds\n"
    "s_add_u32 m0, s38, 28672\n"
    "s_cmp_eq_u32 s81, 0\n"
    "s_cselect_b32 s80, s64, s65\n"
    "s_add_u32 s80, s80, s57\n"
    "buffer_load_dwordx4 v3, s[16:19], s80 offen lds\n"
    "s_add_u32 m0, s38, 131072\n"
    "s_add_u32 s80, s65, s50\n"
    "buffer_load_dwordx4 v3, s[16:19], s80 offen lds\n"
    "s_add_u32 m0, s38, 135168\n"
    "s_add_u32 s80, s65, s52\n"
    "buffer_load_dwordx4 v3, s[16:19], s80 offen lds\n"
    "s_waitcnt vmcnt(11)\n"
    "s_barrier\n"
    "s_add_u32 m0, s38, 139264\n"
    "s_add_u32 s80, s65, s54\n"
    "buffer_load_dwordx4 v3, s[16:19], s80 offen lds\n"
    "s_add_u32 m0, s38, 143360\n"
    "s_add_u32 s80, s65, s56\n"
    "buffer_load_dwordx4 v3, s[16:19], s80 offen lds\n"
    "s_add_u32 m0, s38, 147456\n"
    "s_add_u32 s80, s66, s49\n"
    "buffer_load_dwordx4 v3, s[16:19], s80 offen lds\n"
    "s_add_u32 m0, s38, 151552\n"
    "s_add_u32 s80, s66, s51\n"
    "buffer_load_dwordx4 v3, s[16:19], s80 offen lds\n"
    "s_add_u32 m0, s38, 155648\n"
    "s_add_u32 s80, s66, s53\n"
    "buffer_load_dwordx4 v3, s[16:19], s80 offen lds\n"
    "s_waitcnt vmcnt(12)\n"
    "s_barrier\n"
    "s_add_u32 m0, s38, 159744\n"
    "s_add_u32 s80, s66, s55\n"
    "buffer_load_dwordx4 v3, s[16:19], s80 offen lds\n"
    "s_add_u32 m0, s38, 65536\n"
    "s_cmp_eq_u32 s81, 0\n"
    "s_cselect_b32 s80, s66, s67\n"
    "s_add_u32 s80, s80, s57\n"
    "buffer_load_dwordx4 v3, s[16:19], s80 offen lds\n"
    "s_add_u32 m0, s38, 69632\n"
    "s_add_u32 s80, s67, s50\n"
    "buffer_load_dwordx4 v3, s[16:19], s80 offen lds\n"
    "s_add_u32 m0, s38, 73728\n"
    "s_add_u32 s80, s67, s52\n"
    "buffer_load_dwordx4 v3, s[16:19], s80 offen lds\n"
    "s_add_u32 m0, s38, 77824\n"
    "s_add_u32 s80, s67, s54\n"
    "buffer_load_dwordx4 v3, s[16:19], s80 offen lds\n"
    "s_waitcnt vmcnt(12)\n"
    "s_barrier\n"
    "s_cmp_lt_u32 s10, 2\n"
    "s_cbranch_scc1 L_L19_%=\n"
    "s_add_u32 m0, s38, 81920\n"
    "s_add_u32 s80, s67, s56\n"
    "buffer_load_dwordx4 v3, s[16:19], s80 offen lds\n"
    "s_waitcnt vmcnt(9)\n"
    "s_barrier\n"
    "s_waitcnt vmcnt(4)\n"
    "s_barrier\n"
    "s_waitcnt vmcnt(0)\n"
    "s_barrier\n"
    "s_branch L_end_%=\n"
    "L_L19_%=:\n"
    "s_add_u32 m0, s38, 81920\n"
    "s_add_u32 s80, s67, s56\n"
    "buffer_load_dwordx4 v3, s[16:19], s80 offen lds\n"
    "s_add_u32 m0, s38, 86016\n"
    "s_add_u32 s80, s68, s49\n"
    "buffer_load_dwordx4 v3, s[16:19], s80 offen lds\n"
    "s_add_u32 m0, s38, 90112\n"
    "s_add_u32 s80, s68, s51\n"
    "buffer_load_dwordx4 v3, s[16:19], s80 offen lds\n"
    "s_add_u32 m0, s38, 94208\n"
    "s_add_u32 s80, s68, s53\n"
    "buffer_load_dwordx4 v3, s[16:19], s80 offen lds\n"
    "s_add_u32 m0, s38, 98304\n"
    "s_add_u32 s80, s68, s55\n"
    "buffer_load_dwordx4 v3, s[16:19], s80 offen lds\n"
    "s_waitcnt vmcnt(13)\n"
    "s_barrier\n"
    "s_add_u32 m0, s38, 102400\n"
    "s_cmp_eq_u32 s81, 0\n"
    "s_cselect_b32 s80, s68, s69\n"
    "s_add_u32 s80, s80, s57\n"
    "buffer_load_dwordx4 v3, s[16:19], s80 offen lds\n"
    "s_waitcnt vmcnt(9)\n"
    "s_barrier\n"
    "s_waitcnt vmcnt(0)\n"
    "s_barrier\n"
    "s_branch L_end_%=\n"
    "L_Mbody_%=:\n"
    "s_barrier\n"
    "ds_read2st64_b64 v[112:115], v5 offset0:0 offset1:1\n"
    "ds_read2st64_b64 v[116:119], v5 offset0:2 offset1:3\n"
    "s_waitcnt lgkmcnt(0)\n"
    "v_lshl_or_b32 v116, v118, 8, v116\n"
    "v_lshl_or_b32 v112, v114, 8, v112\n"
    "v_lshl_or_b32 v117, v119, 8, v117\n"
    "v_lshl_or_b32 v113, v115, 8, v113\n"
    "v_lshl_or_b32 v2, v116, 16, v112\n"
    "v_lshl_or_b32 v3, v117, 16, v113\n"
    "ds_read2st64_b64 v[112:115], v5 offset0:4 offset1:5\n"
    "ds_read2st64_b64 v[116:119], v5 offset0:6 offset1:7\n"
    "s_waitcnt lgkmcnt(0)\n"
    "v_lshl_or_b32 v116, v118, 8, v116\n"
    "v_lshl_or_b32 v112, v114, 8, v112\n"
    "v_lshl_or_b32 v117, v119, 8, v117\n"
    "v_lshl_or_b32 v113, v115, 8, v113\n"
    "v_lshl_or_b32 v6, v116, 16, v112\n"
    "v_lshl_or_b32 v7, v117, 16, v113\n"
    "s_barrier\n"
    "ds_read2st64_b64 v[120:123], v5 offset0:32 offset1:33\n"
    "ds_read2st64_b64 v[124:127], v5 offset0:34 offset1:35\n"
    "v_xor_b32_e32 v0, s74, v2\n"
    "v_xor_b32_e32 v4, s74, v3\n"
    "v_perm_b32 v112, v17, v16, v2\n"
    "v_perm_b32 v116, v25, v24, v3\n"
    "v_perm_b32 v113, v21, v20, v2\n"
    "v_perm_b32 v117, v29, v28, v3\n"
    "v_perm_b32 v2, v19, v18, v0\n"
    "v_perm_b32 v3, v27, v26, v4\n"
    "v_perm_b32 v0, v23, v22, v0\n"
    "v_perm_b32 v4, v31, v30, v4\n"
    "v_xor_b32_e32 v2, v112, v2\n"
    "v_xor_b32_e32 v3, v116, v3\n"
    "v_xor_b32_e32 v0, v113, v0\n"
    "v_xor_b32_e32 v4, v117, v4\n"
    "v_perm_b32 v112, v0, v2, s75\n"
    "v_perm_b32 v116, v4, v3, s75\n"
    "v_perm_b32 v113, v0, v2, s76\n"
    "v_perm_b32 v117, v4, v3, s76\n"
    "s_waitcnt lgkmcnt(0)\n"
    "v_lshl_or_b32 v124, v126, 8, v124\n"
    "v_lshl_or_b32 v120, v122, 8, v120\n"
    "v_lshl_or_b32 v125, v127, 8, v125\n"
    "v_lshl_or_b32 v121, v123, 8, v121\n"
    "v_lshl_or_b32 v2, v124, 16, v120\n"
    "v_lshl_or_b32 v3, v125, 16, v121\n"
    "ds_read2st64_b64 v[120:123], v5 offset0:36 offset1:37\n"
    "ds_read2st64_b64 v[124:127], v5 offset0:38 offset1:39\n"
    "v_xor_b32_e32 v0, s74, v6\n"
    "v_xor_b32_e64 v4, s74, v7\n"
    "v_perm_b32 v114, v17, v16, v6\n"
    "v_perm_b32 v118, v25, v24, v7\n"
    "v_perm_b32 v115, v21, v20, v6\n"
    "v_perm_b32 v119, v29, v28, v7\n"
    "v_perm_b32 v6, v19, v18, v0\n"
    "v_perm_b32 v7, v27, v26, v4\n"
    "v_perm_b32 v0, v23, v22, v0\n"
    "v_perm_b32 v4, v31, v30, v4\n"
    "v_xor_b32_e32 v6, v114, v6\n"
    "v_xor_b32_e32 v7, v118, v7\n"
    "v_xor_b32_e32 v0, v115, v0\n"
    "v_xor_b32_e32 v4, v119, v4\n"
    "v_perm_b32 v114, v0, v6, s75\n"
    "v_perm_b32 v118, v4, v7, s75\n"
    "v_perm_b32 v115, v0, v6, s76\n"
    "v_perm_b32 v119, v4, v7, s76\n"
    "s_waitcnt lgkmcnt(0)\n"
    "v_lshl_or_b32 v124, v126, 8, v124\n"
    "v_lshl_or_b32 v120, v122, 8, v120\n"
    "v_lshl_or_b32 v125, v127, 8, v125\n"
    "v_lshl_or_b32 v121, v123, 8, v121\n"
    "v_lshl_or_b32 v6, v124, 16, v120\n"
    "v_lshl_or_b32 v7, v125, 16, v121\n"
    "ds_read_b128 v[8:11], v1 offset:0\n"
    "ds_read_b128 v[12:15], v1 offset:2048\n"
    "ds_read_b128 v[32:35], v1 offset:4096\n"
    "ds_read_b128 v[36:39], v1 offset:6144\n"
    "ds_read_b128 v[40:43], v1 offset:8192\n"
    "ds_read_b128 v[44:47], v1 offset:10240\n"
    "v_xor_b32_e32 v0, s74, v2\n"
    "v_xor_b32_e64 v4, s74, v3\n"
    "v_perm_b32 v120, v17, v16, v2\n"
    "s_waitcnt lgkmcnt(5)\n"
    "v_mfma_f32_16x16x32_bf16 v[48:51], v[8:11], v[112:115], 0\n"
    "v_perm_b32 v124, v25, v24, v3\n"
    "v_perm_b32 v121, v21, v20, v2\n"
    "v_perm_b32 v125, v29, v28, v3\n"
    "v_mfma_f32_16x16x32_bf16 v[52:55], v[8:11], v[116:119], 0\n"
    "ds_read_b128 v[8:11], v1 offset:12288\n"
    "v_perm_b32 v2, v19, v18, v0\n"
    "v_perm_b32 v3, v27, v26, v4\n"
    "v_perm_b32 v0, v23, v22, v0\n"
    "s_waitcnt lgkmcnt(5)\n"
    "v_mfma_f32_16x16x32_bf16 v[56:59], v[12:15], v[112:115], 0\n"
    "v_perm_b32 v4, v31, v30, v4\n"
    "v_xor_b32_e32 v2, v120, v2\n"
    "v_xor_b32_e32 v3, v124, v3\n"
    "v_mfma_f32_16x16x32_bf16 v[60:63], v[12:15], v[116:119], 0\n"
    "ds_read_b128 v[12:15], v1 offset:14336\n"
    "v_xor_b32_e32 v0, v121, v0\n"
    "v_xor_b32_e32 v4, v125, v4\n"
    "s_waitcnt lgkmcnt(5)\n"
    "v_mfma_f32_16x16x32_bf16 v[64:67], v[32:35], v[112:115], 0\n"
    "v_perm_b32 v120, v0, v2, s75\n"
    "v_perm_b32 v124, v4, v3, s75\n"
    "v_mfma_f32_16x16x32_bf16 v[68:71], v[32:35], v[116:119], 0\n"
    "v_xor_b32_e32 v1, 64, v1\n"
    "ds_read_b128 v[32:35], v1 offset:0\n"
    "v_perm_b32 v121, v0, v2, s76\n"
    "v_perm_b32 v125, v4, v3, s76\n"
    "s_waitcnt lgkmcnt(5)\n"
    "v_mfma_f32_16x16x32_bf16 v[72:75], v[36:39], v[112:115], 0\n"
    "v_xor_b32_e32 v0, s74, v6\n"
    "v_xor_b32_e64 v4, s74, v7\n"
    "v_mfma_f32_16x16x32_bf16 v[76:79], v[36:39], v[116:119], 0\n"
    "ds_read_b128 v[36:39], v1 offset:2048\n"
    "v_perm_b32 v122, v17, v16, v6\n"
    "v_perm_b32 v126, v25, v24, v7\n"
    "s_waitcnt lgkmcnt(5)\n"
    "v_mfma_f32_16x16x32_bf16 v[80:83], v[40:43], v[112:115], 0\n"
    "v_perm_b32 v123, v21, v20, v6\n"
    "v_perm_b32 v127, v29, v28, v7\n"
    "v_mfma_f32_16x16x32_bf16 v[84:87], v[40:43], v[116:119], 0\n"
    "ds_read_b128 v[40:43], v1 offset:4096\n"
    "v_perm_b32 v6, v19, v18, v0\n"
    "v_perm_b32 v7, v27, v26, v4\n"
    "s_waitcnt lgkmcnt(5)\n"
    "v_mfma_f32_16x16x32_bf16 v[88:91], v[44:47], v[112:115], 0\n"
    "v_perm_b32 v0, v23, v22, v0\n"
    "v_perm_b32 v4, v31, v30, v4\n"
    "v_mfma_f32_16x16x32_bf16 v[92:95], v[44:47], v[116:119], 0\n"
    "ds_read_b128 v[44:47], v1 offset:6144\n"
    "v_xor_b32_e32 v6, v122, v6\n"
    "v_xor_b32_e32 v7, v126, v7\n"
    "s_waitcnt lgkmcnt(5)\n"
    "v_mfma_f32_16x16x32_bf16 v[96:99], v[8:11], v[112:115], 0\n"
    "v_xor_b32_e32 v0, v123, v0\n"
    "v_xor_b32_e64 v4, v127, v4\n"
    "v_mfma_f32_16x16x32_bf16 v[100:103], v[8:11], v[116:119], 0\n"
    "ds_read_b128 v[8:11], v1 offset:8192\n"
    "v_perm_b32 v122, v0, v6, s75\n"
    "v_perm_b32 v126, v4, v7, s75\n"
    "s_waitcnt lgkmcnt(5)\n"
    "v_mfma_f32_16x16x32_bf16 v[104:107], v[12:15], v[112:115], 0\n"
    "v_perm_b32 v123, v0, v6, s76\n"
    "v_perm_b32 v127, v4, v7, s76\n"
    "v_mfma_f32_16x16x32_bf16 v[108:111], v[12:15], v[116:119], 0\n"
    "ds_read_b128 v[12:15], v1 offset:10240\n"
    "s_waitcnt lgkmcnt(5)\n"
    "v_mfma_f32_16x16x32_bf16 v[48:51], v[32:35], v[120:123], v[48:51]\n"
    "v_mfma_f32_16x16x32_bf16 v[52:55], v[32:35], v[124:127], v[52:55]\n"
    "ds_read_b128 v[32:35], v1 offset:12288\n"
    "s_waitcnt lgkmcnt(5)\n"
    "v_mfma_f32_16x16x32_bf16 v[56:59], v[36:39], v[120:123], v[56:59]\n"
    "v_mfma_f32_16x16x32_bf16 v[60:63], v[36:39], v[124:127], v[60:63]\n"
    "ds_read_b128 v[36:39], v1 offset:14336\n"
    "s_waitcnt lgkmcnt(5)\n"
    "v_mfma_f32_16x16x32_bf16 v[64:67], v[40:43], v[120:123], v[64:67]\n"
    "v_mfma_f32_16x16x32_bf16 v[68:71], v[40:43], v[124:127], v[68:71]\n"
    "s_waitcnt lgkmcnt(4)\n"
    "v_mfma_f32_16x16x32_bf16 v[72:75], v[44:47], v[120:123], v[72:75]\n"
    "v_mfma_f32_16x16x32_bf16 v[76:79], v[44:47], v[124:127], v[76:79]\n"
    "s_waitcnt lgkmcnt(3)\n"
    "v_mfma_f32_16x16x32_bf16 v[80:83], v[8:11], v[120:123], v[80:83]\n"
    "v_mfma_f32_16x16x32_bf16 v[84:87], v[8:11], v[124:127], v[84:87]\n"
    "s_waitcnt lgkmcnt(2)\n"
    "v_mfma_f32_16x16x32_bf16 v[88:91], v[12:15], v[120:123], v[88:91]\n"
    "v_mfma_f32_16x16x32_bf16 v[92:95], v[12:15], v[124:127], v[92:95]\n"
    "s_waitcnt lgkmcnt(1)\n"
    "v_mfma_f32_16x16x32_bf16 v[96:99], v[32:35], v[120:123], v[96:99]\n"
    "v_mfma_f32_16x16x32_bf16 v[100:103], v[32:35], v[124:127], v[100:103]\n"
    "s_waitcnt lgkmcnt(0)\n"
    "v_mfma_f32_16x16x32_bf16 v[104:107], v[36:39], v[120:123], v[104:107]\n"
    "v_mfma_f32_16x16x32_bf16 v[108:111], v[36:39], v[124:127], v[108:111]\n"
    "s_cmp_lt_u32 s10, 2\n"
    "s_cselect_b32 s13, 11, 10\n"
    "s_mul_i32 s80, s9, 52\n"
    "s_lshr_b32 s80, s80, 8\n"
    "s_mul_i32 s80, s80, 5\n"
    "s_sub_u32 s12, s9, s80\n"
    "s_mul_i32 s12, s12, 8\n"
    "s_lshr_b32 s12, s12, 2\n"
    "s_movk_i32 s78, 18432\n"
    "s_add_u32 s58, s12, 0\n"
    "s_sub_u32 s80, s58, s13\n"
    "s_min_u32 s58, s58, s80\n"
    "s_add_u32 s58, s58, 8\n"
    "s_mul_i32 s58, s58, s78\n"
    "s_add_u32 s59, s12, 1\n"
    "s_sub_u32 s80, s59, s13\n"
    "s_min_u32 s59, s59, s80\n"
    "s_add_u32 s59, s59, 8\n"
    "s_mul_i32 s59, s59, s78\n"
    "s_add_u32 s60, s12, 2\n"
    "s_sub_u32 s80, s60, s13\n"
    "s_min_u32 s60, s60, s80\n"
    "s_add_u32 s60, s60, 8\n"
    "s_mul_i32 s60, s60, s78\n"
    "s_add_u32 s61, s12, 3\n"
    "s_sub_u32 s80, s61, s13\n"
    "s_min_u32 s61, s61, s80\n"
    "s_add_u32 s61, s61, 8\n"
    "s_mul_i32 s61, s61, s78\n"
    "s_add_u32 s62, s12, 4\n"
    "s_sub_u32 s80, s62, s13\n"
    "s_min_u32 s62, s62, s80\n"
    "s_add_u32 s62, s62, 8\n"
    "s_mul_i32 s62, s62, s78\n"
    "s_add_u32 s63, s12, 5\n"
    "s_sub_u32 s80, s63, s13\n"
    "s_min_u32 s63, s63, s80\n"
    "s_add_u32 s63, s63, 8\n"
    "s_mul_i32 s63, s63, s78\n"
    "s_add_u32 s64, s12, 6\n"
    "s_sub_u32 s80, s64, s13\n"
    "s_min_u32 s64, s64, s80\n"
    "s_add_u32 s64, s64, 8\n"
    "s_mul_i32 s64, s64, s78\n"
    "s_add_u32 s65, s12, 7\n"
    "s_sub_u32 s80, s65, s13\n"
    "s_min_u32 s65, s65, s80\n"
    "s_add_u32 s65, s65, 8\n"
    "s_mul_i32 s65, s65, s78\n"
    "s_add_u32 s66, s12, 8\n"
    "s_sub_u32 s80, s66, s13\n"
    "s_min_u32 s66, s66, s80\n"
    "s_add_u32 s66, s66, 8\n"
    "s_mul_i32 s66, s66, s78\n"
    "s_add_u32 s67, s12, 9\n"
    "s_sub_u32 s80, s67, s13\n"
    "s_min_u32 s67, s67, s80\n"
    "s_add_u32 s67, s67, 8\n"
    "s_mul_i32 s67, s67, s78\n"
    "s_add_u32 s68, s12, 10\n"
    "s_sub_u32 s80, s68, s13\n"
    "s_min_u32 s68, s68, s80\n"
    "s_add_u32 s68, s68, 8\n"
    "s_mul_i32 s68, s68, s78\n"
    "s_mov_b32 s69, s68\n"
    "s_barrier\n"
    "ds_read2st64_b64 v[128:131], v5 offset0:64 offset1:65\n"
    "ds_read2st64_b64 v[132:135], v5 offset0:66 offset1:67\n"
    "s_waitcnt lgkmcnt(0)\n"
    "v_lshl_or_b32 v132, v134, 8, v132\n"
    "v_lshl_or_b32 v128, v130, 8, v128\n"
    "v_lshl_or_b32 v133, v135, 8, v133\n"
    "v_lshl_or_b32 v129, v131, 8, v129\n"
    "v_lshl_or_b32 v2, v132, 16, v128\n"
    "v_lshl_or_b32 v3, v133, 16, v129\n"
    "ds_read2st64_b64 v[128:131], v5 offset0:68 offset1:69\n"
    "ds_read2st64_b64 v[132:135], v5 offset0:70 offset1:71\n"
    "s_waitcnt lgkmcnt(0)\n"
    "v_lshl_or_b32 v132, v134, 8, v132\n"
    "v_lshl_or_b32 v128, v130, 8, v128\n"
    "v_lshl_or_b32 v133, v135, 8, v133\n"
    "v_lshl_or_b32 v129, v131, 8, v129\n"
    "v_lshl_or_b32 v6, v132, 16, v128\n"
    "v_lshl_or_b32 v7, v133, 16, v129\n"
    "s_barrier\n"
    "ds_read2st64_b64 v[136:139], v5 offset0:96 offset1:97\n"
    "ds_read2st64_b64 v[140:143], v5 offset0:98 offset1:99\n"
    "v_xor_b32_e32 v0, s74, v2\n"
    "v_xor_b32_e32 v4, s74, v3\n"
    "v_perm_b32 v128, v17, v16, v2\n"
    "v_perm_b32 v132, v25, v24, v3\n"
    "v_perm_b32 v129, v21, v20, v2\n"
    "v_perm_b32 v133, v29, v28, v3\n"
    "v_perm_b32 v2, v19, v18, v0\n"
    "v_perm_b32 v3, v27, v26, v4\n"
    "v_perm_b32 v0, v23, v22, v0\n"
    "v_perm_b32 v4, v31, v30, v4\n"
    "v_xor_b32_e32 v2, v128, v2\n"
    "v_xor_b32_e32 v3, v132, v3\n"
    "v_xor_b32_e32 v0, v129, v0\n"
    "v_xor_b32_e32 v4, v133, v4\n"
    "v_perm_b32 v128, v0, v2, s75\n"
    "v_perm_b32 v132, v4, v3, s75\n"
    "v_perm_b32 v129, v0, v2, s76\n"
    "v_perm_b32 v133, v4, v3, s76\n"
    "s_waitcnt lgkmcnt(0)\n"
    "v_lshl_or_b32 v140, v142, 8, v140\n"
    "v_lshl_or_b32 v136, v138, 8, v136\n"
    "v_lshl_or_b32 v141, v143, 8, v141\n"
    "v_lshl_or_b32 v137, v139, 8, v137\n"
    "v_lshl_or_b32 v2, v140, 16, v136\n"
    "v_lshl_or_b32 v3, v141, 16, v137\n"
    "ds_read2st64_b64 v[136:139], v5 offset0:100 offset1:101\n"
    "ds_read2st64_b64 v[140:143], v5 offset0:102 offset1:103\n"
    "v_xor_b32_e32 v0, s74, v6\n"
    "v_xor_b32_e64 v4, s74, v7\n"
    "v_perm_b32 v130, v17, v16, v6\n"
    "v_perm_b32 v134, v25, v24, v7\n"
    "v_perm_b32 v131, v21, v20, v6\n"
    "v_perm_b32 v135, v29, v28, v7\n"
    "v_perm_b32 v6, v19, v18, v0\n"
    "v_perm_b32 v7, v27, v26, v4\n"
    "v_perm_b32 v0, v23, v22, v0\n"
    "v_perm_b32 v4, v31, v30, v4\n"
    "v_xor_b32_e32 v6, v130, v6\n"
    "v_xor_b32_e32 v7, v134, v7\n"
    "v_xor_b32_e32 v0, v131, v0\n"
    "v_xor_b32_e32 v4, v135, v4\n"
    "v_perm_b32 v130, v0, v6, s75\n"
    "v_perm_b32 v134, v4, v7, s75\n"
    "v_perm_b32 v131, v0, v6, s76\n"
    "v_perm_b32 v135, v4, v7, s76\n"
    "s_waitcnt lgkmcnt(0)\n"
    "v_lshl_or_b32 v140, v142, 8, v140\n"
    "v_lshl_or_b32 v136, v138, 8, v136\n"
    "v_lshl_or_b32 v141, v143, 8, v141\n"
    "v_lshl_or_b32 v137, v139, 8, v137\n"
    "v_lshl_or_b32 v6, v140, 16, v136\n"
    "v_lshl_or_b32 v7, v141, 16, v137\n"
    "v_xor_b32_e32 v1, 64, v1\n"
    "ds_read_b128 v[40:43], v1 offset:16384\n"
    "ds_read_b128 v[44:47], v1 offset:18432\n"
    "ds_read_b128 v[8:11], v1 offset:20480\n"
    "ds_read_b128 v[12:15], v1 offset:22528\n"
    "ds_read_b128 v[32:35], v1 offset:24576\n"
    "ds_read_b128 v[36:39], v1 offset:26624\n"
    "s_barrier\n"
    "ds_read2st64_b64 v[144:147], v5 offset0:0 offset1:1\n"
    "ds_read2st64_b64 v[148:151], v5 offset0:2 offset1:3\n"
    "v_xor_b32_e32 v0, s74, v2\n"
    "v_xor_b32_e32 v4, s74, v3\n"
    "s_waitcnt lgkmcnt(7)\n"
    "v_mfma_f32_16x16x32_bf16 v[48:51], v[40:43], v[128:131], v[48:51]\n"
    "v_perm_b32 v136, v17, v16, v2\n"
    "v_perm_b32 v140, v25, v24, v3\n"
    "v_perm_b32 v137, v21, v20, v2\n"
    "v_perm_b32 v141, v29, v28, v3\n"
    "v_mfma_f32_16x16x32_bf16 v[52:55], v[40:43], v[132:135], v[52:55]\n"
    "ds_read_b128 v[40:43], v1 offset:28672\n"
    "v_perm_b32 v2, v19, v18, v0\n"
    "v_perm_b32 v3, v27, v26, v4\n"
    "v_perm_b32 v0, v23, v22, v0\n"
    "v_perm_b32 v4, v31, v30, v4\n"
    "s_waitcnt lgkmcnt(7)\n"
    "v_mfma_f32_16x16x32_bf16 v[56:59], v[44:47], v[128:131], v[56:59]\n"
    "v_xor_b32_e32 v2, v136, v2\n"
    "v_xor_b32_e32 v3, v140, v3\n"
    "v_xor_b32_e32 v0, v137, v0\n"
    "v_xor_b32_e64 v4, v141, v4\n"
    "v_mfma_f32_16x16x32_bf16 v[60:63], v[44:47], v[132:135], v[60:63]\n"
    "ds_read_b128 v[44:47], v1 offset:30720\n"
    "v_perm_b32 v136, v0, v2, s75\n"
    "v_perm_b32 v140, v4, v3, s75\n"
    "v_perm_b32 v137, v0, v2, s76\n"
    "s_waitcnt lgkmcnt(7)\n"
    "v_mfma_f32_16x16x32_bf16 v[64:67], v[8:11], v[128:131], v[64:67]\n"
    "v_perm_b32 v141, v4, v3, s76\n"
    "s_waitcnt lgkmcnt(2)\n"
    "v_lshl_or_b32 v148, v150, 8, v148\n"
    "v_lshl_or_b32 v144, v146, 8, v144\n"
    "v_mfma_f32_16x16x32_bf16 v[68:71], v[8:11], v[132:135], v[68:71]\n"
    "v_xor_b32_e64 v1, 64, v1\n"
    "ds_read_b128 v[8:11], v1 offset:16384\n"
    "v_lshl_or_b32 v149, v151, 8, v149\n"
    "v_lshl_or_b32 v145, v147, 8, v145\n"
    "v_lshl_or_b32 v2, v148, 16, v144\n"
    "v_mfma_f32_16x16x32_bf16 v[72:75], v[12:15], v[128:131], v[72:75]\n"
    "v_lshl_or_b32 v3, v149, 16, v145\n"
    "ds_read2st64_b64 v[144:147], v5 offset0:4 offset1:5\n"
    "ds_read2st64_b64 v[148:151], v5 offset0:6 offset1:7\n"
    "v_mfma_f32_16x16x32_bf16 v[76:79], v[12:15], v[132:135], v[76:79]\n"
    "ds_read_b128 v[12:15], v1 offset:18432\n"
    "v_xor_b32_e32 v0, s74, v6\n"
    "v_xor_b32_e32 v4, s74, v7\n"
    "v_perm_b32 v138, v17, v16, v6\n"
    "v_mfma_f32_16x16x32_bf16 v[80:83], v[32:35], v[128:131], v[80:83]\n"
    "v_perm_b32 v142, v25, v24, v7\n"
    "v_perm_b32 v139, v21, v20, v6\n"
    "v_perm_b32 v143, v29, v28, v7\n"
    "v_mfma_f32_16x16x32_bf16 v[84:87], v[32:35], v[132:135], v[84:87]\n"
    "ds_read_b128 v[32:35], v1 offset:20480\n"
    "v_perm_b32 v6, v19, v18, v0\n"
    "v_perm_b32 v7, v27, v26, v4\n"
    "v_perm_b32 v0, v23, v22, v0\n"
    "v_mfma_f32_16x16x32_bf16 v[88:91], v[36:39], v[128:131], v[88:91]\n"
    "v_perm_b32 v4, v31, v30, v4\n"
    "v_xor_b32_e32 v6, v138, v6\n"
    "v_xor_b32_e32 v7, v142, v7\n"
    "v_mfma_f32_16x16x32_bf16 v[92:95], v[36:39], v[132:135], v[92:95]\n"
    "ds_read_b128 v[36:39], v1 offset:22528\n"
    "v_xor_b32_e32 v0, v139, v0\n"
    "v_xor_b32_e32 v4, v143, v4\n"
    "v_perm_b32 v138, v0, v6, s75\n"
    "s_waitcnt lgkmcnt(7)\n"
    "v_mfma_f32_16x16x32_bf16 v[96:99], v[40:43], v[128:131], v[96:99]\n"
    "v_perm_b32 v142, v4, v7, s75\n"
    "v_perm_b32 v139, v0, v6, s76\n"
    "v_perm_b32 v143, v4, v7, s76\n"
    "v_mfma_f32_16x16x32_bf16 v[100:103], v[40:43], v[132:135], v[100:103]\n"
    "ds_read_b128 v[40:43], v1 offset:24576\n"
    "s_waitcnt lgkmcnt(4)\n"
    "v_lshl_or_b32 v148, v150, 8, v148\n"
    "v_lshl_or_b32 v144, v146, 8, v144\n"
    "v_lshl_or_b32 v149, v151, 8, v149\n"
    "v_mfma_f32_16x16x32_bf16 v[104:107], v[44:47], v[128:131], v[104:107]\n"
    "v_lshl_or_b32 v145, v147, 8, v145\n"
    "v_lshl_or_b32 v6, v148, 16, v144\n"
    "v_lshl_or_b32 v7, v149, 16, v145\n"
    "v_mfma_f32_16x16x32_bf16 v[108:111], v[44:47], v[132:135], v[108:111]\n"
    "ds_read_b128 v[44:47], v1 offset:26624\n"
    "s_barrier\n"
    "ds_read2st64_b64 v[152:155], v5 offset0:32 offset1:33\n"
    "ds_read2st64_b64 v[156:159], v5 offset0:34 offset1:35\n"
    "v_xor_b32_e32 v0, s74, v2\n"
    "v_xor_b32_e64 v4, s74, v3\n"
    "v_mfma_f32_16x16x32_bf16 v[48:51], v[8:11], v[136:139], v[48:51]\n"
    "v_perm_b32 v144, v17, v16, v2\n"
    "v_perm_b32 v148, v25, v24, v3\n"
    "v_perm_b32 v145, v21, v20, v2\n"
    "v_perm_b32 v149, v29, v28, v3\n"
    "v_mfma_f32_16x16x32_bf16 v[52:55], v[8:11], v[140:143], v[52:55]\n"
    "ds_read_b128 v[8:11], v1 offset:28672\n"
    "v_perm_b32 v2, v19, v18, v0\n"
    "v_perm_b32 v3, v27, v26, v4\n"
    "v_perm_b32 v0, v23, v22, v0\n"
    "v_perm_b32 v4, v31, v30, v4\n"
    "s_waitcnt lgkmcnt(7)\n"
    "v_mfma_f32_16x16x32_bf16 v[56:59], v[12:15], v[136:139], v[56:59]\n"
    "v_xor_b32_e32 v2, v144, v2\n"
    "v_xor_b32_e32 v3, v148, v3\n"
    "v_xor_b32_e32 v0, v145, v0\n"
    "v_xor_b32_e64 v4, v149, v4\n"
    "v_mfma_f32_16x16x32_bf16 v[60:63], v[12:15], v[140:143], v[60:63]\n"
    "ds_read_b128 v[12:15], v1 offset:30720\n"
    "v_perm_b32 v144, v0, v2, s75\n"
    "v_perm_b32 v148, v4, v3, s75\n"
    "v_perm_b32 v145, v0, v2, s76\n"
    "s_waitcnt lgkmcnt(7)\n"
    "v_mfma_f32_16x16x32_bf16 v[64:67], v[32:35], v[136:139], v[64:67]\n"
    "v_perm_b32 v149, v4, v3, s76\n"
    "s_waitcnt lgkmcnt(2)\n"
    "v_lshl_or_b32 v156, v158, 8, v156\n"
    "v_lshl_or_b32 v152, v154, 8, v152\n"
    "v_mfma_f32_16x16x32_bf16 v[68:71], v[32:35], v[140:143], v[68:71]\n"
    "v_xor_b32_e64 v1, 64, v1\n"
    "ds_read_b128 v[32:35], v1 offset:32768\n"
    "v_lshl_or_b32 v157, v159, 8, v157\n"
    "v_lshl_or_b32 v153, v155, 8, v153\n"
    "v_lshl_or_b32 v2, v156, 16, v152\n"
    "v_mfma_f32_16x16x32_bf16 v[72:75], v[36:39], v[136:139], v[72:75]\n"
    "v_lshl_or_b32 v3, v157, 16, v153\n"
    "ds_read2st64_b64 v[152:155], v5 offset0:36 offset1:37\n"
    "ds_read2st64_b64 v[156:159], v5 offset0:38 offset1:39\n"
    "v_mfma_f32_16x16x32_bf16 v[76:79], v[36:39], v[140:143], v[76:79]\n"
    "ds_read_b128 v[36:39], v1 offset:34816\n"
    "v_xor_b32_e32 v0, s74, v6\n"
    "v_xor_b32_e32 v4, s74, v7\n"
    "v_perm_b32 v146, v17, v16, v6\n"
    "v_mfma_f32_16x16x32_bf16 v[80:83], v[40:43], v[136:139], v[80:83]\n"
    "v_perm_b32 v150, v25, v24, v7\n"
    "v_perm_b32 v147, v21, v20, v6\n"
    "v_perm_b32 v151, v29, v28, v7\n"
    "v_mfma_f32_16x16x32_bf16 v[84:87], v[40:43], v[140:143], v[84:87]\n"
    "ds_read_b128 v[40:43], v1 offset:36864\n"
    "v_perm_b32 v6, v19, v18, v0\n"
    "v_perm_b32 v7, v27, v26, v4\n"
    "v_perm_b32 v0, v23, v22, v0\n"
    "v_mfma_f32_16x16x32_bf16 v[88:91], v[44:47], v[136:139], v[88:91]\n"
    "v_perm_b32 v4, v31, v30, v4\n"
    "v_xor_b32_e32 v6, v146, v6\n"
    "v_xor_b32_e32 v7, v150, v7\n"
    "v_mfma_f32_16x16x32_bf16 v[92:95], v[44:47], v[140:143], v[92:95]\n"
    "ds_read_b128 v[44:47], v1 offset:38912\n"
    "v_xor_b32_e32 v0, v147, v0\n"
    "v_xor_b32_e32 v4, v151, v4\n"
    "v_perm_b32 v146, v0, v6, s75\n"
    "s_waitcnt lgkmcnt(7)\n"
    "v_mfma_f32_16x16x32_bf16 v[96:99], v[8:11], v[136:139], v[96:99]\n"
    "v_perm_b32 v150, v4, v7, s75\n"
    "v_perm_b32 v147, v0, v6, s76\n"
    "v_perm_b32 v151, v4, v7, s76\n"
    "v_mfma_f32_16x16x32_bf16 v[100:103], v[8:11], v[140:143], v[100:103]\n"
    "ds_read_b128 v[8:11], v1 offset:40960\n"
    "s_waitcnt lgkmcnt(4)\n"
    "v_lshl_or_b32 v156, v158, 8, v156\n"
    "v_lshl_or_b32 v152, v154, 8, v152\n"
    "v_lshl_or_b32 v157, v159, 8, v157\n"
    "v_mfma_f32_16x16x32_bf16 v[104:107], v[12:15], v[136:139], v[104:107]\n"
    "v_lshl_or_b32 v153, v155, 8, v153\n"
    "v_lshl_or_b32 v6, v156, 16, v152\n"
    "v_lshl_or_b32 v7, v157, 16, v153\n"
    "v_mfma_f32_16x16x32_bf16 v[108:111], v[12:15], v[140:143], v[108:111]\n"
    "ds_read_b128 v[12:15], v1 offset:43008\n"
    "s_barrier\n"
    "ds_read2st64_b64 v[160:163], v5 offset0:64 offset1:65\n"
    "ds_read2st64_b64 v[164:167], v5 offset0:66 offset1:67\n"
    "v_xor_b32_e32 v0, s74, v2\n"
    "v_xor_b32_e64 v4, s74, v3\n"
    "v_mfma_f32_16x16x32_bf16 v[48:51], v[32:35], v[144:147], v[48:51]\n"
    "v_perm_b32 v152, v17, v16, v2\n"
    "v_perm_b32 v156, v25, v24, v3\n"
    "v_perm_b32 v153, v21, v20, v2\n"
    "v_perm_b32 v157, v29, v28, v3\n"
    "v_mfma_f32_16x16x32_bf16 v[52:55], v[32:35], v[148:151], v[52:55]\n"
    "ds_read_b128 v[32:35], v1 offset:45056\n"
    "v_perm_b32 v2, v19, v18, v0\n"
    "v_perm_b32 v3, v27, v26, v4\n"
    "v_perm_b32 v0, v23, v22, v0\n"
    "v_perm_b32 v4, v31, v30, v4\n"
    "s_waitcnt lgkmcnt(7)\n"
    "v_mfma_f32_16x16x32_bf16 v[56:59], v[36:39], v[144:147], v[56:59]\n"
    "v_xor_b32_e32 v2, v152, v2\n"
    "v_xor_b32_e32 v3, v156, v3\n"
    "v_xor_b32_e32 v0, v153, v0\n"
    "v_xor_b32_e64 v4, v157, v4\n"
    "v_mfma_f32_16x16x32_bf16 v[60:63], v[36:39], v[148:151], v[60:63]\n"
    "ds_read_b128 v[36:39], v1 offset:47104\n"
    "v_perm_b32 v152, v0, v2, s75\n"
    "v_perm_b32 v156, v4, v3, s75\n"
    "v_perm_b32 v153, v0, v2, s76\n"
    "s_waitcnt lgkmcnt(7)\n"
    "v_mfma_f32_16x16x32_bf16 v[64:67], v[40:43], v[144:147], v[64:67]\n"
    "v_perm_b32 v157, v4, v3, s76\n"
    "s_waitcnt lgkmcnt(2)\n"
    "v_lshl_or_b32 v164, v166, 8, v164\n"
    "v_lshl_or_b32 v160, v162, 8, v160\n"
    "v_mfma_f32_16x16x32_bf16 v[68:71], v[40:43], v[148:151], v[68:71]\n"
    "v_xor_b32_e64 v1, 64, v1\n"
    "ds_read_b128 v[40:43], v1 offset:32768\n"
    "v_lshl_or_b32 v165, v167, 8, v165\n"
    "v_lshl_or_b32 v161, v163, 8, v161\n"
    "v_lshl_or_b32 v2, v164, 16, v160\n"
    "v_mfma_f32_16x16x32_bf16 v[72:75], v[44:47], v[144:147], v[72:75]\n"
    "v_lshl_or_b32 v3, v165, 16, v161\n"
    "ds_read2st64_b64 v[160:163], v5 offset0:68 offset1:69\n"
    "ds_read2st64_b64 v[164:167], v5 offset0:70 offset1:71\n"
    "v_mfma_f32_16x16x32_bf16 v[76:79], v[44:47], v[148:151], v[76:79]\n"
    "ds_read_b128 v[44:47], v1 offset:34816\n"
    "v_xor_b32_e32 v0, s74, v6\n"
    "v_xor_b32_e32 v4, s74, v7\n"
    "v_perm_b32 v154, v17, v16, v6\n"
    "v_mfma_f32_16x16x32_bf16 v[80:83], v[8:11], v[144:147], v[80:83]\n"
    "v_perm_b32 v158, v25, v24, v7\n"
    "v_perm_b32 v155, v21, v20, v6\n"
    "v_perm_b32 v159, v29, v28, v7\n"
    "v_mfma_f32_16x16x32_bf16 v[84:87], v[8:11], v[148:151], v[84:87]\n"
    "ds_read_b128 v[8:11], v1 offset:36864\n"
    "v_perm_b32 v6, v19, v18, v0\n"
    "v_perm_b32 v7, v27, v26, v4\n"
    "v_perm_b32 v0, v23, v22, v0\n"
    "v_mfma_f32_16x16x32_bf16 v[88:91], v[12:15], v[144:147], v[88:91]\n"
    "v_perm_b32 v4, v31, v30, v4\n"
    "v_xor_b32_e32 v6, v154, v6\n"
    "v_xor_b32_e32 v7, v158, v7\n"
    "v_mfma_f32_16x16x32_bf16 v[92:95], v[12:15], v[148:151], v[92:95]\n"
    "ds_read_b128 v[12:15], v1 offset:38912\n"
    "v_xor_b32_e32 v0, v155, v0\n"
    "v_xor_b32_e32 v4, v159, v4\n"
    "v_perm_b32 v154, v0, v6, s75\n"
    "s_waitcnt lgkmcnt(7)\n"
    "v_mfma_f32_16x16x32_bf16 v[96:99], v[32:35], v[144:147], v[96:99]\n"
    "v_perm_b32 v158, v4, v7, s75\n"
    "v_perm_b32 v155, v0, v6, s76\n"
    "v_perm_b32 v159, v4, v7, s76\n"
    "v_mfma_f32_16x16x32_bf16 v[100:103], v[32:35], v[148:151], v[100:103]\n"
    "ds_read_b128 v[32:35], v1 offset:40960\n"
    "s_waitcnt lgkmcnt(4)\n"
    "v_lshl_or_b32 v164, v166, 8, v164\n"
    "v_lshl_or_b32 v160, v162, 8, v160\n"
    "v_lshl_or_b32 v165, v167, 8, v165\n"
    "v_mfma_f32_16x16x32_bf16 v[104:107], v[36:39], v[144:147], v[104:107]\n"
    "v_lshl_or_b32 v161, v163, 8, v161\n"
    "v_lshl_or_b32 v6, v164, 16, v160\n"
    "v_lshl_or_b32 v7, v165, 16, v161\n"
    "v_mfma_f32_16x16x32_bf16 v[108:111], v[36:39], v[148:151], v[108:111]\n"
    "ds_read_b128 v[36:39], v1 offset:43008\n"
    "s_barrier\n"
    "ds_read2st64_b64 v[168:171], v5 offset0:96 offset1:97\n"
    "ds_read2st64_b64 v[172:175], v5 offset0:98 offset1:99\n"
    "v_xor_b32_e32 v0, s74, v2\n"
    "v_xor_b32_e64 v4, s74, v3\n"
    "v_mfma_f32_16x16x32_bf16 v[48:51], v[40:43], v[152:155], v[48:51]\n"
    "v_perm_b32 v160, v17, v16, v2\n"
    "v_perm_b32 v164, v25, v24, v3\n"
    "v_perm_b32 v161, v21, v20, v2\n"
    "v_perm_b32 v165, v29, v28, v3\n"
    "v_mfma_f32_16x16x32_bf16 v[52:55], v[40:43], v[156:159], v[52:55]\n"
    "ds_read_b128 v[40:43], v1 offset:45056\n"
    "v_perm_b32 v2, v19, v18, v0\n"
    "v_perm_b32 v3, v27, v26, v4\n"
    "v_perm_b32 v0, v23, v22, v0\n"
    "v_perm_b32 v4, v31, v30, v4\n"
    "s_waitcnt lgkmcnt(7)\n"
    "v_mfma_f32_16x16x32_bf16 v[56:59], v[44:47], v[152:155], v[56:59]\n"
    "v_xor_b32_e32 v2, v160, v2\n"
    "v_xor_b32_e32 v3, v164, v3\n"
    "v_xor_b32_e32 v0, v161, v0\n"
    "v_xor_b32_e64 v4, v165, v4\n"
    "v_mfma_f32_16x16x32_bf16 v[60:63], v[44:47], v[156:159], v[60:63]\n"
    "ds_read_b128 v[44:47], v1 offset:47104\n"
    "v_perm_b32 v160, v0, v2, s75\n"
    "v_perm_b32 v164, v4, v3, s75\n"
    "v_perm_b32 v161, v0, v2, s76\n"
    "s_waitcnt lgkmcnt(7)\n"
    "v_mfma_f32_16x16x32_bf16 v[64:67], v[8:11], v[152:155], v[64:67]\n"
    "v_perm_b32 v165, v4, v3, s76\n"
    "s_waitcnt lgkmcnt(2)\n"
    "v_lshl_or_b32 v172, v174, 8, v172\n"
    "v_lshl_or_b32 v168, v170, 8, v168\n"
    "v_mfma_f32_16x16x32_bf16 v[68:71], v[8:11], v[156:159], v[68:71]\n"
    "v_xor_b32_e64 v1, 64, v1\n"
    "ds_read_b128 v[8:11], v1 offset:0\n"
    "v_lshl_or_b32 v173, v175, 8, v173\n"
    "v_lshl_or_b32 v169, v171, 8, v169\n"
    "v_lshl_or_b32 v2, v172, 16, v168\n"
    "v_mfma_f32_16x16x32_bf16 v[72:75], v[12:15], v[152:155], v[72:75]\n"
    "v_lshl_or_b32 v3, v173, 16, v169\n"
    "ds_read2st64_b64 v[168:171], v5 offset0:100 offset1:101\n"
    "ds_read2st64_b64 v[172:175], v5 offset0:102 offset1:103\n"
    "v_mfma_f32_16x16x32_bf16 v[76:79], v[12:15], v[156:159], v[76:79]\n"
    "ds_read_b128 v[12:15], v1 offset:2048\n"
    "v_xor_b32_e32 v0, s74, v6\n"
    "v_xor_b32_e32 v4, s74, v7\n"
    "v_perm_b32 v162, v17, v16, v6\n"
    "v_mfma_f32_16x16x32_bf16 v[80:83], v[32:35], v[152:155], v[80:83]\n"
    "v_perm_b32 v166, v25, v24, v7\n"
    "v_perm_b32 v163, v21, v20, v6\n"
    "v_perm_b32 v167, v29, v28, v7\n"
    "v_mfma_f32_16x16x32_bf16 v[84:87], v[32:35], v[156:159], v[84:87]\n"
    "ds_read_b128 v[32:35], v1 offset:4096\n"
    "v_perm_b32 v6, v19, v18, v0\n"
    "v_perm_b32 v7, v27, v26, v4\n"
    "v_perm_b32 v0, v23, v22, v0\n"
    "v_mfma_f32_16x16x32_bf16 v[88:91], v[36:39], v[152:155], v[88:91]\n"
    "v_perm_b32 v4, v31, v30, v4\n"
    "v_xor_b32_e32 v6, v162, v6\n"
    "v_xor_b32_e32 v7, v166, v7\n"
    "v_mfma_f32_16x16x32_bf16 v[92:95], v[36:39], v[156:159], v[92:95]\n"
    "ds_read_b128 v[36:39], v1 offset:6144\n"
    "v_xor_b32_e32 v0, v163, v0\n"
    "v_xor_b32_e32 v4, v167, v4\n"
    "v_perm_b32 v162, v0, v6, s75\n"
    "s_waitcnt lgkmcnt(7)\n"
    "v_mfma_f32_16x16x32_bf16 v[96:99], v[40:43], v[152:155], v[96:99]\n"
    "v_perm_b32 v166, v4, v7, s75\n"
    "v_perm_b32 v163, v0, v6, s76\n"
    "v_perm_b32 v167, v4, v7, s76\n"
    "v_mfma_f32_16x16x32_bf16 v[100:103], v[40:43], v[156:159], v[100:103]\n"
    "ds_read_b128 v[40:43], v1 offset:8192\n"
    "s_waitcnt lgkmcnt(4)\n"
    "v_lshl_or_b32 v172, v174, 8, v172\n"
    "v_lshl_or_b32 v168, v170, 8, v168\n"
    "v_lshl_or_b32 v173, v175, 8, v173\n"
    "v_mfma_f32_16x16x32_bf16 v[104:107], v[44:47], v[152:155], v[104:107]\n"
    "v_lshl_or_b32 v169, v171, 8, v169\n"
    "v_lshl_or_b32 v6, v172, 16, v168\n"
    "v_lshl_or_b32 v7, v173, 16, v169\n"
    "v_mfma_f32_16x16x32_bf16 v[108:111], v[44:47], v[156:159], v[108:111]\n"
    "ds_read_b128 v[44:47], v1 offset:10240\n"
    "s_barrier\n"
    "ds_read2st64_b64 v[176:179], v5 offset0:0 offset1:1\n"
    "ds_read2st64_b64 v[180:183], v5 offset0:2 offset1:3\n"
    "v_xor_b32_e32 v0, s74, v2\n"
    "v_xor_b32_e64 v4, s74, v3\n"
    "v_mfma_f32_16x16x32_bf16 v[48:51], v[8:11], v[160:163], v[48:51]\n"
    "v_perm_b32 v168, v17, v16, v2\n"
    "v_perm_b32 v172, v25, v24, v3\n"
    "v_perm_b32 v169, v21, v20, v2\n"
    "v_perm_b32 v173, v29, v28, v3\n"
    "v_mfma_f32_16x16x32_bf16 v[52:55], v[8:11], v[164:167], v[52:55]\n"
    "ds_read_b128 v[8:11], v1 offset:12288\n"
    "v_perm_b32 v2, v19, v18, v0\n"
    "v_perm_b32 v3, v27, v26, v4\n"
    "v_perm_b32 v0, v23, v22, v0\n"
    "v_perm_b32 v4, v31, v30, v4\n"
    "s_waitcnt lgkmcnt(7)\n"
    "v_mfma_f32_16x16x32_bf16 v[56:59], v[12:15], v[160:163], v[56:59]\n"
    "v_xor_b32_e32 v2, v168, v2\n"
    "v_xor_b32_e32 v3, v172, v3\n"
    "v_xor_b32_e32 v0, v169, v0\n"
    "v_xor_b32_e64 v4, v173, v4\n"
    "v_mfma_f32_16x16x32_bf16 v[60:63], v[12:15], v[164:167], v[60:63]\n"
    "ds_read_b128 v[12:15], v1 offset:14336\n"
    "v_perm_b32 v168, v0, v2, s75\n"
    "v_perm_b32 v172, v4, v3, s75\n"
    "v_perm_b32 v169, v0, v2, s76\n"
    "s_waitcnt lgkmcnt(7)\n"
    "v_mfma_f32_16x16x32_bf16 v[64:67], v[32:35], v[160:163], v[64:67]\n"
    "v_perm_b32 v173, v4, v3, s76\n"
    "s_waitcnt lgkmcnt(2)\n"
    "v_lshl_or_b32 v180, v182, 8, v180\n"
    "v_lshl_or_b32 v176, v178, 8, v176\n"
    "v_mfma_f32_16x16x32_bf16 v[68:71], v[32:35], v[164:167], v[68:71]\n"
    "v_xor_b32_e64 v1, 64, v1\n"
    "ds_read_b128 v[32:35], v1 offset:0\n"
    "v_lshl_or_b32 v181, v183, 8, v181\n"
    "v_lshl_or_b32 v177, v179, 8, v177\n"
    "v_lshl_or_b32 v2, v180, 16, v176\n"
    "v_mfma_f32_16x16x32_bf16 v[72:75], v[36:39], v[160:163], v[72:75]\n"
    "v_lshl_or_b32 v3, v181, 16, v177\n"
    "ds_read2st64_b64 v[176:179], v5 offset0:4 offset1:5\n"
    "ds_read2st64_b64 v[180:183], v5 offset0:6 offset1:7\n"
    "v_mfma_f32_16x16x32_bf16 v[76:79], v[36:39], v[164:167], v[76:79]\n"
    "ds_read_b128 v[36:39], v1 offset:2048\n"
    "v_xor_b32_e32 v0, s74, v6\n"
    "v_xor_b32_e32 v4, s74, v7\n"
    "v_perm_b32 v170, v17, v16, v6\n"
    "v_mfma_f32_16x16x32_bf16 v[80:83], v[40:43], v[160:163], v[80:83]\n"
    "v_perm_b32 v174, v25, v24, v7\n"
    "v_perm_b32 v171, v21, v20, v6\n"
    "v_perm_b32 v175, v29, v28, v7\n"
    "v_mfma_f32_16x16x32_bf16 v[84:87], v[40:43], v[164:167], v[84:87]\n"
    "ds_read_b128 v[40:43], v1 offset:4096\n"
    "v_perm_b32 v6, v19, v18, v0\n"
    "v_perm_b32 v7, v27, v26, v4\n"
    "v_perm_b32 v0, v23, v22, v0\n"
    "v_mfma_f32_16x16x32_bf16 v[88:91], v[44:47], v[160:163], v[88:91]\n"
    "v_perm_b32 v4, v31, v30, v4\n"
    "v_xor_b32_e32 v6, v170, v6\n"
    "v_xor_b32_e32 v7, v174, v7\n"
    "v_mfma_f32_16x16x32_bf16 v[92:95], v[44:47], v[164:167], v[92:95]\n"
    "ds_read_b128 v[44:47], v1 offset:6144\n"
    "v_xor_b32_e32 v0, v171, v0\n"
    "v_xor_b32_e32 v4, v175, v4\n"
    "v_perm_b32 v170, v0, v6, s75\n"
    "s_waitcnt lgkmcnt(7)\n"
    "v_mfma_f32_16x16x32_bf16 v[96:99], v[8:11], v[160:163], v[96:99]\n"
    "v_perm_b32 v174, v4, v7, s75\n"
    "v_perm_b32 v171, v0, v6, s76\n"
    "v_perm_b32 v175, v4, v7, s76\n"
    "v_mfma_f32_16x16x32_bf16 v[100:103], v[8:11], v[164:167], v[100:103]\n"
    "ds_read_b128 v[8:11], v1 offset:8192\n"
    "s_waitcnt lgkmcnt(4)\n"
    "v_lshl_or_b32 v180, v182, 8, v180\n"
    "v_lshl_or_b32 v176, v178, 8, v176\n"
    "v_lshl_or_b32 v181, v183, 8, v181\n"
    "v_mfma_f32_16x16x32_bf16 v[104:107], v[12:15], v[160:163], v[104:107]\n"
    "v_lshl_or_b32 v177, v179, 8, v177\n"
    "v_lshl_or_b32 v6, v180, 16, v176\n"
    "v_lshl_or_b32 v7, v181, 16, v177\n"
    "v_mfma_f32_16x16x32_bf16 v[108:111], v[12:15], v[164:167], v[108:111]\n"
    "ds_read_b128 v[12:15], v1 offset:10240\n"
    "s_barrier\n"
    "ds_read2st64_b64 v[184:187], v5 offset0:32 offset1:33\n"
    "ds_read2st64_b64 v[188:191], v5 offset0:34 offset1:35\n"
    "v_xor_b32_e32 v0, s74, v2\n"
    "v_xor_b32_e64 v4, s74, v3\n"
    "v_mfma_f32_16x16x32_bf16 v[48:51], v[32:35], v[168:171], v[48:51]\n"
    "v_perm_b32 v176, v17, v16, v2\n"
    "v_perm_b32 v180, v25, v24, v3\n"
    "v_perm_b32 v177, v21, v20, v2\n"
    "v_perm_b32 v181, v29, v28, v3\n"
    "v_mfma_f32_16x16x32_bf16 v[52:55], v[32:35], v[172:175], v[52:55]\n"
    "ds_read_b128 v[32:35], v1 offset:12288\n"
    "v_perm_b32 v2, v19, v18, v0\n"
    "v_perm_b32 v3, v27, v26, v4\n"
    "v_perm_b32 v0, v23, v22, v0\n"
    "v_perm_b32 v4, v31, v30, v4\n"
    "s_waitcnt lgkmcnt(7)\n"
    "v_mfma_f32_16x16x32_bf16 v[56:59], v[36:39], v[168:171], v[56:59]\n"
    "v_xor_b32_e32 v2, v176, v2\n"
    "v_xor_b32_e32 v3, v180, v3\n"
    "v_xor_b32_e32 v0, v177, v0\n"
    "v_xor_b32_e64 v4, v181, v4\n"
    "v_mfma_f32_16x16x32_bf16 v[60:63], v[36:39], v[172:175], v[60:63]\n"
    "ds_read_b128 v[36:39], v1 offset:14336\n"
    "v_perm_b32 v176, v0, v2, s75\n"
    "v_perm_b32 v180, v4, v3, s75\n"
    "v_perm_b32 v177, v0, v2, s76\n"
    "s_waitcnt lgkmcnt(7)\n"
    "v_mfma_f32_16x16x32_bf16 v[64:67], v[40:43], v[168:171], v[64:67]\n"
    "v_perm_b32 v181, v4, v3, s76\n"
    "s_waitcnt lgkmcnt(2)\n"
    "v_lshl_or_b32 v188, v190, 8, v188\n"
    "v_lshl_or_b32 v184, v186, 8, v184\n"
    "v_mfma_f32_16x16x32_bf16 v[68:71], v[40:43], v[172:175], v[68:71]\n"
    "v_xor_b32_e64 v1, 64, v1\n"
    "ds_read_b128 v[40:43], v1 offset:16384\n"
    "v_lshl_or_b32 v189, v191, 8, v189\n"
    "v_lshl_or_b32 v185, v187, 8, v185\n"
    "v_lshl_or_b32 v2, v188, 16, v184\n"
    "v_mfma_f32_16x16x32_bf16 v[72:75], v[44:47], v[168:171], v[72:75]\n"
    "v_lshl_or_b32 v3, v189, 16, v185\n"
    "ds_read2st64_b64 v[184:187], v5 offset0:36 offset1:37\n"
    "ds_read2st64_b64 v[188:191], v5 offset0:38 offset1:39\n"
    "v_mfma_f32_16x16x32_bf16 v[76:79], v[44:47], v[172:175], v[76:79]\n"
    "ds_read_b128 v[44:47], v1 offset:18432\n"
    "v_xor_b32_e32 v0, s74, v6\n"
    "v_xor_b32_e32 v4, s74, v7\n"
    "v_perm_b32 v178, v17, v16, v6\n"
    "v_mfma_f32_16x16x32_bf16 v[80:83], v[8:11], v[168:171], v[80:83]\n"
    "v_perm_b32 v182, v25, v24, v7\n"
    "v_perm_b32 v179, v21, v20, v6\n"
    "v_perm_b32 v183, v29, v28, v7\n"
    "v_mfma_f32_16x16x32_bf16 v[84:87], v[8:11], v[172:175], v[84:87]\n"
    "ds_read_b128 v[8:11], v1 offset:20480\n"
    "v_perm_b32 v6, v19, v18, v0\n"
    "v_perm_b32 v7, v27, v26, v4\n"
    "v_perm_b32 v0, v23, v22, v0\n"
    "v_mfma_f32_16x16x32_bf16 v[88:91], v[12:15], v[168:171], v[88:91]\n"
    "v_perm_b32 v4, v31, v30, v4\n"
    "v_xor_b32_e32 v6, v178, v6\n"
    "v_xor_b32_e32 v7, v182, v7\n"
    "v_mfma_f32_16x16x32_bf16 v[92:95], v[12:15], v[172:175], v[92:95]\n"
    "ds_read_b128 v[12:15], v1 offset:22528\n"
    "v_xor_b32_e32 v0, v179, v0\n"
    "v_xor_b32_e32 v4, v183, v4\n"
    "v_perm_b32 v178, v0, v6, s75\n"
    "s_waitcnt lgkmcnt(7)\n"
    "v_mfma_f32_16x16x32_bf16 v[96:99], v[32:35], v[168:171], v[96:99]\n"
    "v_perm_b32 v182, v4, v7, s75\n"
    "v_perm_b32 v179, v0, v6, s76\n"
    "v_perm_b32 v183, v4, v7, s76\n"
    "v_mfma_f32_16x16x32_bf16 v[100:103], v[32:35], v[172:175], v[100:103]\n"
    "ds_read_b128 v[32:35], v1 offset:24576\n"
    "s_waitcnt lgkmcnt(4)\n"
    "v_lshl_or_b32 v188, v190, 8, v188\n"
    "v_lshl_or_b32 v184, v186, 8, v184\n"
    "v_lshl_or_b32 v189, v191, 8, v189\n"
    "v_mfma_f32_16x16x32_bf16 v[104:107], v[36:39], v[168:171], v[104:107]\n"
    "v_lshl_or_b32 v185, v187, 8, v185\n"
    "v_lshl_or_b32 v6, v188, 16, v184\n"
    "v_lshl_or_b32 v7, v189, 16, v185\n"
    "v_mfma_f32_16x16x32_bf16 v[108:111], v[36:39], v[172:175], v[108:111]\n"
    "ds_read_b128 v[36:39], v1 offset:26624\n"
    "s_barrier\n"
    "ds_read2st64_b64 v[192:195], v5 offset0:64 offset1:65\n"
    "ds_read2st64_b64 v[196:199], v5 offset0:66 offset1:67\n"
    "v_xor_b32_e32 v0, s74, v2\n"
    "v_xor_b32_e64 v4, s74, v3\n"
    "v_mfma_f32_16x16x32_bf16 v[48:51], v[40:43], v[176:179], v[48:51]\n"
    "v_perm_b32 v184, v17, v16, v2\n"
    "v_perm_b32 v188, v25, v24, v3\n"
    "v_perm_b32 v185, v21, v20, v2\n"
    "v_perm_b32 v189, v29, v28, v3\n"
    "v_mfma_f32_16x16x32_bf16 v[52:55], v[40:43], v[180:183], v[52:55]\n"
    "ds_read_b128 v[40:43], v1 offset:28672\n"
    "v_perm_b32 v2, v19, v18, v0\n"
    "v_perm_b32 v3, v27, v26, v4\n"
    "v_perm_b32 v0, v23, v22, v0\n"
    "v_perm_b32 v4, v31, v30, v4\n"
    "s_waitcnt lgkmcnt(7)\n"
    "v_mfma_f32_16x16x32_bf16 v[56:59], v[44:47], v[176:179], v[56:59]\n"
    "v_xor_b32_e32 v2, v184, v2\n"
    "v_xor_b32_e32 v3, v188, v3\n"
    "v_xor_b32_e32 v0, v185, v0\n"
    "v_xor_b32_e64 v4, v189, v4\n"
    "v_mfma_f32_16x16x32_bf16 v[60:63], v[44:47], v[180:183], v[60:63]\n"
    "ds_read_b128 v[44:47], v1 offset:30720\n"
    "v_perm_b32 v184, v0, v2, s75\n"
    "v_perm_b32 v188, v4, v3, s75\n"
    "v_perm_b32 v185, v0, v2, s76\n"
    "s_waitcnt lgkmcnt(7)\n"
    "v_mfma_f32_16x16x32_bf16 v[64:67], v[8:11], v[176:179], v[64:67]\n"
    "v_perm_b32 v189, v4, v3, s76\n"
    "s_waitcnt lgkmcnt(2)\n"
    "v_lshl_or_b32 v196, v198, 8, v196\n"
    "v_lshl_or_b32 v192, v194, 8, v192\n"
    "v_mfma_f32_16x16x32_bf16 v[68:71], v[8:11], v[180:183], v[68:71]\n"
    "v_xor_b32_e64 v1, 64, v1\n"
    "ds_read_b128 v[8:11], v1 offset:16384\n"
    "v_lshl_or_b32 v197, v199, 8, v197\n"
    "v_lshl_or_b32 v193, v195, 8, v193\n"
    "v_lshl_or_b32 v2, v196, 16, v192\n"
    "v_mfma_f32_16x16x32_bf16 v[72:75], v[12:15], v[176:179], v[72:75]\n"
    "v_lshl_or_b32 v3, v197, 16, v193\n"
    "ds_read2st64_b64 v[192:195], v5 offset0:68 offset1:69\n"
    "ds_read2st64_b64 v[196:199], v5 offset0:70 offset1:71\n"
    "v_mfma_f32_16x16x32_bf16 v[76:79], v[12:15], v[180:183], v[76:79]\n"
    "ds_read_b128 v[12:15], v1 offset:18432\n"
    "v_xor_b32_e32 v0, s74, v6\n"
    "v_xor_b32_e32 v4, s74, v7\n"
    "v_perm_b32 v186, v17, v16, v6\n"
    "v_mfma_f32_16x16x32_bf16 v[80:83], v[32:35], v[176:179], v[80:83]\n"
    "v_perm_b32 v190, v25, v24, v7\n"
    "v_perm_b32 v187, v21, v20, v6\n"
    "v_perm_b32 v191, v29, v28, v7\n"
    "v_mfma_f32_16x16x32_bf16 v[84:87], v[32:35], v[180:183], v[84:87]\n"
    "ds_read_b128 v[32:35], v1 offset:20480\n"
    "v_perm_b32 v6, v19, v18, v0\n"
    "v_perm_b32 v7, v27, v26, v4\n"
    "v_perm_b32 v0, v23, v22, v0\n"
    "v_mfma_f32_16x16x32_bf16 v[88:91], v[36:39], v[176:179], v[88:91]\n"
    "v_perm_b32 v4, v31, v30, v4\n"
    "v_xor_b32_e32 v6, v186, v6\n"
    "v_xor_b32_e32 v7, v190, v7\n"
    "v_mfma_f32_16x16x32_bf16 v[92:95], v[36:39], v[180:183], v[92:95]\n"
    "ds_read_b128 v[36:39], v1 offset:22528\n"
    "v_xor_b32_e32 v0, v187, v0\n"
    "v_xor_b32_e32 v4, v191, v4\n"
    "v_perm_b32 v186, v0, v6, s75\n"
    "s_waitcnt lgkmcnt(7)\n"
    "v_mfma_f32_16x16x32_bf16 v[96:99], v[40:43], v[176:179], v[96:99]\n"
    "v_perm_b32 v190, v4, v7, s75\n"
    "v_perm_b32 v187, v0, v6, s76\n"
    "v_perm_b32 v191, v4, v7, s76\n"
    "v_mfma_f32_16x16x32_bf16 v[100:103], v[40:43], v[180:183], v[100:103]\n"
    "ds_read_b128 v[40:43], v1 offset:24576\n"
    "s_waitcnt lgkmcnt(4)\n"
    "v_lshl_or_b32 v196, v198, 8, v196\n"
    "v_lshl_or_b32 v192, v194, 8, v192\n"
    "v_lshl_or_b32 v197, v199, 8, v197\n"
    "v_mfma_f32_16x16x32_bf16 v[104:107], v[44:47], v[176:179], v[104:107]\n"
    "v_lshl_or_b32 v193, v195, 8, v193\n"
    "v_lshl_or_b32 v6, v196, 16, v192\n"
    "v_lshl_or_b32 v7, v197, 16, v193\n"
    "v_mfma_f32_16x16x32_bf16 v[108:111], v[44:47], v[180:183], v[108:111]\n"
    "ds_read_b128 v[44:47], v1 offset:26624\n"
    "s_barrier\n"
    "ds_read2st64_b64 v[200:203], v5 offset0:96 offset1:97\n"
    "s_waitcnt vmcnt(0)\n"
    "ds_read2st64_b64 v[204:207], v5 offset0:98 offset1:99\n"
    "v_xor_b32_e32 v0, s74, v2\n"
    "v_xor_b32_e32 v4, s74, v3\n"
    "v_mfma_f32_16x16x32_bf16 v[48:51], v[8:11], v[184:187], v[48:51]\n"
    "v_perm_b32 v192, v17, v16, v2\n"
    "v_perm_b32 v196, v25, v24, v3\n"
    "v_perm_b32 v193, v21, v20, v2\n"
    "v_perm_b32 v197, v29, v28, v3\n"
    "v_mfma_f32_16x16x32_bf16 v[52:55], v[8:11], v[188:191], v[52:55]\n"
    "ds_read_b128 v[8:11], v1 offset:28672\n"
    "v_perm_b32 v2, v19, v18, v0\n"
    "v_perm_b32 v3, v27, v26, v4\n"
    "v_perm_b32 v0, v23, v22, v0\n"
    "v_perm_b32 v4, v31, v30, v4\n"
    "s_waitcnt lgkmcnt(7)\n"
    "v_mfma_f32_16x16x32_bf16 v[56:59], v[12:15], v[184:187], v[56:59]\n"
    "v_xor_b32_e32 v2, v192, v2\n"
    "v_xor_b32_e32 v3, v196, v3\n"
    "v_xor_b32_e32 v0, v193, v0\n"
    "v_xor_b32_e64 v4, v197, v4\n"
    "v_mfma_f32_16x16x32_bf16 v[60:63], v[12:15], v[188:191], v[60:63]\n"
    "ds_read_b128 v[12:15], v1 offset:30720\n"
    "v_perm_b32 v192, v0, v2, s75\n"
    "v_perm_b32 v196, v4, v3, s75\n"
    "v_perm_b32 v193, v0, v2, s76\n"
    "s_waitcnt lgkmcnt(7)\n"
    "v_mfma_f32_16x16x32_bf16 v[64:67], v[32:35], v[184:187], v[64:67]\n"
    "v_perm_b32 v197, v4, v3, s76\n"
    "s_waitcnt lgkmcnt(2)\n"
    "v_lshl_or_b32 v204, v206, 8, v204\n"
    "v_lshl_or_b32 v200, v202, 8, v200\n"
    "v_mfma_f32_16x16x32_bf16 v[68:71], v[32:35], v[188:191], v[68:71]\n"
    "v_xor_b32_e64 v1, 64, v1\n"
    "ds_read_b128 v[32:35], v1 offset:32768\n"
    "v_lshl_or_b32 v205, v207, 8, v205\n"
    "v_lshl_or_b32 v201, v203, 8, v201\n"
    "v_lshl_or_b32 v2, v204, 16, v200\n"
    "v_mfma_f32_16x16x32_bf16 v[72:75], v[36:39], v[184:187], v[72:75]\n"
    "v_lshl_or_b32 v3, v205, 16, v201\n"
    "ds_read2st64_b64 v[200:203], v5 offset0:100 offset1:101\n"
    "ds_read2st64_b64 v[204:207], v5 offset0:102 offset1:103\n"
    "v_mfma_f32_16x16x32_bf16 v[76:79], v[36:39], v[188:191], v[76:79]\n"
    "ds_read_b128 v[36:39], v1 offset:34816\n"
    "v_xor_b32_e32 v0, s74, v6\n"
    "v_xor_b32_e32 v4, s74, v7\n"
    "v_perm_b32 v194, v17, v16, v6\n"
    "v_mfma_f32_16x16x32_bf16 v[80:83], v[40:43], v[184:187], v[80:83]\n"
    "v_perm_b32 v198, v25, v24, v7\n"
    "v_perm_b32 v195, v21, v20, v6\n"
    "v_perm_b32 v199, v29, v28, v7\n"
    "v_mfma_f32_16x16x32_bf16 v[84:87], v[40:43], v[188:191], v[84:87]\n"
    "ds_read_b128 v[40:43], v1 offset:36864\n"
    "v_perm_b32 v6, v19, v18, v0\n"
    "v_perm_b32 v7, v27, v26, v4\n"
    "v_perm_b32 v0, v23, v22, v0\n"
    "v_mfma_f32_16x16x32_bf16 v[88:91], v[44:47], v[184:187], v[88:91]\n"
    "v_perm_b32 v4, v31, v30, v4\n"
    "v_xor_b32_e32 v6, v194, v6\n"
    "v_xor_b32_e32 v7, v198, v7\n"
    "v_mfma_f32_16x16x32_bf16 v[92:95], v[44:47], v[188:191], v[92:95]\n"
    "ds_read_b128 v[44:47], v1 offset:38912\n"
    "v_xor_b32_e32 v0, v195, v0\n"
    "v_xor_b32_e32 v4, v199, v4\n"
    "v_perm_b32 v194, v0, v6, s75\n"
    "s_waitcnt lgkmcnt(7)\n"
    "v_mfma_f32_16x16x32_bf16 v[96:99], v[8:11], v[184:187], v[96:99]\n"
    "v_perm_b32 v198, v4, v7, s75\n"
    "v_perm_b32 v195, v0, v6, s76\n"
    "v_perm_b32 v199, v4, v7, s76\n"
    "v_mfma_f32_16x16x32_bf16 v[100:103], v[8:11], v[188:191], v[100:103]\n"
    "ds_read_b128 v[8:11], v1 offset:40960\n"
    "s_waitcnt lgkmcnt(4)\n"
    "v_lshl_or_b32 v204, v206, 8, v204\n"
    "v_lshl_or_b32 v200, v202, 8, v200\n"
    "v_lshl_or_b32 v205, v207, 8, v205\n"
    "v_mfma_f32_16x16x32_bf16 v[104:107], v[12:15], v[184:187], v[104:107]\n"
    "v_lshl_or_b32 v201, v203, 8, v201\n"
    "v_lshl_or_b32 v6, v204, 16, v200\n"
    "v_lshl_or_b32 v7, v205, 16, v201\n"
    "v_mfma_f32_16x16x32_bf16 v[108:111], v[12:15], v[188:191], v[108:111]\n"
    "ds_read_b128 v[12:15], v1 offset:43008\n"
    "s_barrier\n"
    "ds_read2st64_b64 v[208:211], v5 offset0:0 offset1:1\n"
    "ds_read2st64_b64 v[212:215], v5 offset0:2 offset1:3\n"
    "v_xor_b32_e32 v0, s74, v2\n"
    "v_xor_b32_e64 v4, s74, v3\n"
    "v_mfma_f32_16x16x32_bf16 v[48:51], v[32:35], v[192:195], v[48:51]\n"
    "v_perm_b32 v200, v17, v16, v2\n"
    "v_perm_b32 v204, v25, v24, v3\n"
    "v_perm_b32 v201, v21, v20, v2\n"
    "v_perm_b32 v205, v29, v28, v3\n"
    "v_mfma_f32_16x16x32_bf16 v[52:55], v[32:35], v[196:199], v[52:55]\n"
    "ds_read_b128 v[32:35], v1 offset:45056\n"
    "v_perm_b32 v2, v19, v18, v0\n"
    "v_perm_b32 v3, v27, v26, v4\n"
    "v_perm_b32 v0, v23, v22, v0\n"
    "v_perm_b32 v4, v31, v30, v4\n"
    "s_waitcnt lgkmcnt(7)\n"
    "v_mfma_f32_16x16x32_bf16 v[56:59], v[36:39], v[192:195], v[56:59]\n"
    "v_xor_b32_e32 v2, v200, v2\n"
    "v_xor_b32_e32 v3, v204, v3\n"
    "v_xor_b32_e32 v0, v201, v0\n"
    "v_xor_b32_e64 v4, v205, v4\n"
    "v_mfma_f32_16x16x32_bf16 v[60:63], v[36:39], v[196:199], v[60:63]\n"
    "ds_read_b128 v[36:39], v1 offset:47104\n"
    "v_perm_b32 v200, v0, v2, s75\n"
    "v_perm_b32 v204, v4, v3, s75\n"
    "v_perm_b32 v201, v0, v2, s76\n"
    "s_waitcnt lgkmcnt(7)\n"
    "v_mfma_f32_16x16x32_bf16 v[64:67], v[40:43], v[192:195], v[64:67]\n"
    "v_perm_b32 v205, v4, v3, s76\n"
    "s_waitcnt lgkmcnt(2)\n"
    "v_lshl_or_b32 v212, v214, 8, v212\n"
    "v_lshl_or_b32 v208, v210, 8, v208\n"
    "v_mfma_f32_16x16x32_bf16 v[68:71], v[40:43], v[196:199], v[68:71]\n"
    "v_xor_b32_e64 v1, 64, v1\n"
    "ds_read_b128 v[40:43], v1 offset:32768\n"
    "v_lshl_or_b32 v213, v215, 8, v213\n"
    "v_lshl_or_b32 v209, v211, 8, v209\n"
    "v_lshl_or_b32 v2, v212, 16, v208\n"
    "v_mfma_f32_16x16x32_bf16 v[72:75], v[44:47], v[192:195], v[72:75]\n"
    "v_lshl_or_b32 v3, v213, 16, v209\n"
    "ds_read2st64_b64 v[208:211], v5 offset0:4 offset1:5\n"
    "ds_read2st64_b64 v[212:215], v5 offset0:6 offset1:7\n"
    "v_mfma_f32_16x16x32_bf16 v[76:79], v[44:47], v[196:199], v[76:79]\n"
    "ds_read_b128 v[44:47], v1 offset:34816\n"
    "v_xor_b32_e32 v0, s74, v6\n"
    "v_xor_b32_e32 v4, s74, v7\n"
    "v_perm_b32 v202, v17, v16, v6\n"
    "v_mfma_f32_16x16x32_bf16 v[80:83], v[8:11], v[192:195], v[80:83]\n"
    "v_perm_b32 v206, v25, v24, v7\n"
    "v_perm_b32 v203, v21, v20, v6\n"
    "v_perm_b32 v207, v29, v28, v7\n"
    "v_mfma_f32_16x16x32_bf16 v[84:87], v[8:11], v[196:199], v[84:87]\n"
    "ds_read_b128 v[8:11], v1 offset:36864\n"
    "v_perm_b32 v6, v19, v18, v0\n"
    "v_perm_b32 v7, v27, v26, v4\n"
    "v_perm_b32 v0, v23, v22, v0\n"
    "v_mfma_f32_16x16x32_bf16 v[88:91], v[12:15], v[192:195], v[88:91]\n"
    "v_perm_b32 v4, v31, v30, v4\n"
    "v_xor_b32_e32 v6, v202, v6\n"
    "v_xor_b32_e32 v7, v206, v7\n"
    "v_mfma_f32_16x16x32_bf16 v[92:95], v[12:15], v[196:199], v[92:95]\n"
    "ds_read_b128 v[12:15], v1 offset:38912\n"
    "v_xor_b32_e32 v0, v203, v0\n"
    "v_xor_b32_e32 v4, v207, v4\n"
    "v_perm_b32 v202, v0, v6, s75\n"
    "s_waitcnt lgkmcnt(7)\n"
    "v_mfma_f32_16x16x32_bf16 v[96:99], v[32:35], v[192:195], v[96:99]\n"
    "v_perm_b32 v206, v4, v7, s75\n"
    "v_perm_b32 v203, v0, v6, s76\n"
    "v_perm_b32 v207, v4, v7, s76\n"
    "v_mfma_f32_16x16x32_bf16 v[100:103], v[32:35], v[196:199], v[100:103]\n"
    "ds_read_b128 v[32:35], v1 offset:40960\n"
    "s_waitcnt lgkmcnt(4)\n"
    "v_lshl_or_b32 v212, v214, 8, v212\n"
    "v_lshl_or_b32 v208, v210, 8, v208\n"
    "v_lshl_or_b32 v213, v215, 8, v213\n"
    "v_mfma_f32_16x16x32_bf16 v[104:107], v[36:39], v[192:195], v[104:107]\n"
    "v_lshl_or_b32 v209, v211, 8, v209\n"
    "v_lshl_or_b32 v6, v212, 16, v208\n"
    "v_lshl_or_b32 v7, v213, 16, v209\n"
    "v_mfma_f32_16x16x32_bf16 v[108:111], v[36:39], v[196:199], v[108:111]\n"
    "ds_read_b128 v[36:39], v1 offset:43008\n"
    "s_barrier\n"
    "ds_read2st64_b64 v[216:219], v5 offset0:32 offset1:33\n"
    "ds_read2st64_b64 v[220:223], v5 offset0:34 offset1:35\n"
    "v_xor_b32_e32 v0, s74, v2\n"
    "v_xor_b32_e64 v4, s74, v3\n"
    "v_mfma_f32_16x16x32_bf16 v[48:51], v[40:43], v[200:203], v[48:51]\n"
    "v_perm_b32 v208, v17, v16, v2\n"
    "v_perm_b32 v212, v25, v24, v3\n"
    "v_perm_b32 v209, v21, v20, v2\n"
    "v_perm_b32 v213, v29, v28, v3\n"
    "v_mfma_f32_16x16x32_bf16 v[52:55], v[40:43], v[204:207], v[52:55]\n"
    "ds_read_b128 v[40:43], v1 offset:45056\n"
    "v_perm_b32 v2, v19, v18, v0\n"
    "v_perm_b32 v3, v27, v26, v4\n"
    "v_perm_b32 v0, v23, v22, v0\n"
    "v_perm_b32 v4, v31, v30, v4\n"
    "s_waitcnt lgkmcnt(7)\n"
    "v_mfma_f32_16x16x32_bf16 v[56:59], v[44:47], v[200:203], v[56:59]\n"
    "v_xor_b32_e32 v2, v208, v2\n"
    "v_xor_b32_e32 v3, v212, v3\n"
    "v_xor_b32_e32 v0, v209, v0\n"
    "v_xor_b32_e64 v4, v213, v4\n"
    "v_mfma_f32_16x16x32_bf16 v[60:63], v[44:47], v[204:207], v[60:63]\n"
    "ds_read_b128 v[44:47], v1 offset:47104\n"
    "v_perm_b32 v208, v0, v2, s75\n"
    "v_perm_b32 v212, v4, v3, s75\n"
    "v_perm_b32 v209, v0, v2, s76\n"
    "s_waitcnt lgkmcnt(7)\n"
    "v_mfma_f32_16x16x32_bf16 v[64:67], v[8:11], v[200:203], v[64:67]\n"
    "v_perm_b32 v213, v4, v3, s76\n"
    "s_waitcnt lgkmcnt(2)\n"
    "v_lshl_or_b32 v220, v222, 8, v220\n"
    "v_lshl_or_b32 v216, v218, 8, v216\n"
    "v_mfma_f32_16x16x32_bf16 v[68:71], v[8:11], v[204:207], v[68:71]\n"
    "v_xor_b32_e64 v1, 64, v1\n"
    "ds_read_b128 v[8:11], v1 offset:0\n"
    "v_lshl_or_b32 v221, v223, 8, v221\n"
    "v_lshl_or_b32 v217, v219, 8, v217\n"
    "v_lshl_or_b32 v2, v220, 16, v216\n"
    "v_mfma_f32_16x16x32_bf16 v[72:75], v[12:15], v[200:203], v[72:75]\n"
    "v_lshl_or_b32 v3, v221, 16, v217\n"
    "ds_read2st64_b64 v[216:219], v5 offset0:36 offset1:37\n"
    "ds_read2st64_b64 v[220:223], v5 offset0:38 offset1:39\n"
    "v_mfma_f32_16x16x32_bf16 v[76:79], v[12:15], v[204:207], v[76:79]\n"
    "ds_read_b128 v[12:15], v1 offset:2048\n"
    "v_xor_b32_e32 v0, s74, v6\n"
    "v_xor_b32_e32 v4, s74, v7\n"
    "v_perm_b32 v210, v17, v16, v6\n"
    "v_mfma_f32_16x16x32_bf16 v[80:83], v[32:35], v[200:203], v[80:83]\n"
    "v_perm_b32 v214, v25, v24, v7\n"
    "v_perm_b32 v211, v21, v20, v6\n"
    "v_perm_b32 v215, v29, v28, v7\n"
    "v_mfma_f32_16x16x32_bf16 v[84:87], v[32:35], v[204:207], v[84:87]\n"
    "ds_read_b128 v[32:35], v1 offset:4096\n"
    "v_perm_b32 v6, v19, v18, v0\n"
    "v_perm_b32 v7, v27, v26, v4\n"
    "v_perm_b32 v0, v23, v22, v0\n"
    "v_mfma_f32_16x16x32_bf16 v[88:91], v[36:39], v[200:203], v[88:91]\n"
    "v_perm_b32 v4, v31, v30, v4\n"
    "v_xor_b32_e32 v6, v210, v6\n"
    "v_xor_b32_e32 v7, v214, v7\n"
    "v_mfma_f32_16x16x32_bf16 v[92:95], v[36:39], v[204:207], v[92:95]\n"
    "ds_read_b128 v[36:39], v1 offset:6144\n"
    "v_xor_b32_e32 v0, v211, v0\n"
    "v_xor_b32_e32 v4, v215, v4\n"
    "v_perm_b32 v210, v0, v6, s75\n"
    "s_waitcnt lgkmcnt(7)\n"
    "v_mfma_f32_16x16x32_bf16 v[96:99], v[40:43], v[200:203], v[96:99]\n"
    "v_perm_b32 v214, v4, v7, s75\n"
    "v_perm_b32 v211, v0, v6, s76\n"
    "v_perm_b32 v215, v4, v7, s76\n"
    "v_mfma_f32_16x16x32_bf16 v[100:103], v[40:43], v[204:207], v[100:103]\n"
    "ds_read_b128 v[40:43], v1 offset:8192\n"
    "s_waitcnt lgkmcnt(4)\n"
    "v_lshl_or_b32 v220, v222, 8, v220\n"
    "v_lshl_or_b32 v216, v218, 8, v216\n"
    "v_lshl_or_b32 v221, v223, 8, v221\n"
    "v_mfma_f32_16x16x32_bf16 v[104:107], v[44:47], v[200:203], v[104:107]\n"
    "v_lshl_or_b32 v217, v219, 8, v217\n"
    "v_lshl_or_b32 v6, v220, 16, v216\n"
    "v_lshl_or_b32 v7, v221, 16, v217\n"
    "v_mfma_f32_16x16x32_bf16 v[108:111], v[44:47], v[204:207], v[108:111]\n"
    "ds_read_b128 v[44:47], v1 offset:10240\n"
    "s_barrier\n"
    "ds_read2st64_b64 v[224:227], v5 offset0:64 offset1:65\n"
    "ds_read2st64_b64 v[228:231], v5 offset0:66 offset1:67\n"
    "v_xor_b32_e32 v0, s74, v2\n"
    "v_xor_b32_e64 v4, s74, v3\n"
    "v_mfma_f32_16x16x32_bf16 v[48:51], v[8:11], v[208:211], v[48:51]\n"
    "v_perm_b32 v216, v17, v16, v2\n"
    "v_perm_b32 v220, v25, v24, v3\n"
    "v_perm_b32 v217, v21, v20, v2\n"
    "v_perm_b32 v221, v29, v28, v3\n"
    "v_mfma_f32_16x16x32_bf16 v[52:55], v[8:11], v[212:215], v[52:55]\n"
    "ds_read_b128 v[8:11], v1 offset:12288\n"
    "v_perm_b32 v2, v19, v18, v0\n"
    "v_perm_b32 v3, v27, v26, v4\n"
    "v_perm_b32 v0, v23, v22, v0\n"
    "v_perm_b32 v4, v31, v30, v4\n"
    "s_waitcnt lgkmcnt(7)\n"
    "v_mfma_f32_16x16x32_bf16 v[56:59], v[12:15], v[208:211], v[56:59]\n"
    "v_xor_b32_e32 v2, v216, v2\n"
    "v_xor_b32_e32 v3, v220, v3\n"
    "v_xor_b32_e32 v0, v217, v0\n"
    "v_xor_b32_e64 v4, v221, v4\n"
    "v_mfma_f32_16x16x32_bf16 v[60:63], v[12:15], v[212:215], v[60:63]\n"
    "ds_read_b128 v[12:15], v1 offset:14336\n"
    "v_perm_b32 v216, v0, v2, s75\n"
    "v_perm_b32 v220, v4, v3, s75\n"
    "v_perm_b32 v217, v0, v2, s76\n"
    "s_waitcnt lgkmcnt(7)\n"
    "v_mfma_f32_16x16x32_bf16 v[64:67], v[32:35], v[208:211], v[64:67]\n"
    "v_perm_b32 v221, v4, v3, s76\n"
    "s_waitcnt lgkmcnt(2)\n"
    "v_lshl_or_b32 v228, v230, 8, v228\n"
    "v_lshl_or_b32 v224, v226, 8, v224\n"
    "v_mfma_f32_16x16x32_bf16 v[68:71], v[32:35], v[212:215], v[68:71]\n"
    "v_xor_b32_e64 v1, 64, v1\n"
    "ds_read_b128 v[32:35], v1 offset:0\n"
    "v_lshl_or_b32 v229, v231, 8, v229\n"
    "v_lshl_or_b32 v225, v227, 8, v225\n"
    "v_lshl_or_b32 v2, v228, 16, v224\n"
    "v_mfma_f32_16x16x32_bf16 v[72:75], v[36:39], v[208:211], v[72:75]\n"
    "v_lshl_or_b32 v3, v229, 16, v225\n"
    "ds_read2st64_b64 v[224:227], v5 offset0:68 offset1:69\n"
    "ds_read2st64_b64 v[228:231], v5 offset0:70 offset1:71\n"
    "v_mfma_f32_16x16x32_bf16 v[76:79], v[36:39], v[212:215], v[76:79]\n"
    "ds_read_b128 v[36:39], v1 offset:2048\n"
    "v_xor_b32_e32 v0, s74, v6\n"
    "v_xor_b32_e32 v4, s74, v7\n"
    "v_perm_b32 v218, v17, v16, v6\n"
    "v_mfma_f32_16x16x32_bf16 v[80:83], v[40:43], v[208:211], v[80:83]\n"
    "v_perm_b32 v222, v25, v24, v7\n"
    "v_perm_b32 v219, v21, v20, v6\n"
    "v_perm_b32 v223, v29, v28, v7\n"
    "v_mfma_f32_16x16x32_bf16 v[84:87], v[40:43], v[212:215], v[84:87]\n"
    "ds_read_b128 v[40:43], v1 offset:4096\n"
    "v_perm_b32 v6, v19, v18, v0\n"
    "v_perm_b32 v7, v27, v26, v4\n"
    "v_perm_b32 v0, v23, v22, v0\n"
    "v_mfma_f32_16x16x32_bf16 v[88:91], v[44:47], v[208:211], v[88:91]\n"
    "v_perm_b32 v4, v31, v30, v4\n"
    "v_xor_b32_e32 v6, v218, v6\n"
    "v_xor_b32_e32 v7, v222, v7\n"
    "v_mfma_f32_16x16x32_bf16 v[92:95], v[44:47], v[212:215], v[92:95]\n"
    "ds_read_b128 v[44:47], v1 offset:6144\n"
    "v_xor_b32_e32 v0, v219, v0\n"
    "v_xor_b32_e32 v4, v223, v4\n"
    "v_perm_b32 v218, v0, v6, s75\n"
    "s_waitcnt lgkmcnt(7)\n"
    "v_mfma_f32_16x16x32_bf16 v[96:99], v[8:11], v[208:211], v[96:99]\n"
    "v_perm_b32 v222, v4, v7, s75\n"
    "v_perm_b32 v219, v0, v6, s76\n"
    "v_perm_b32 v223, v4, v7, s76\n"
    "v_mfma_f32_16x16x32_bf16 v[100:103], v[8:11], v[212:215], v[100:103]\n"
    "ds_read_b128 v[8:11], v1 offset:8192\n"
    "s_waitcnt lgkmcnt(4)\n"
    "v_lshl_or_b32 v228, v230, 8, v228\n"
    "v_lshl_or_b32 v224, v226, 8, v224\n"
    "v_lshl_or_b32 v229, v231, 8, v229\n"
    "v_mfma_f32_16x16x32_bf16 v[104:107], v[12:15], v[208:211], v[104:107]\n"
    "v_lshl_or_b32 v225, v227, 8, v225\n"
    "v_lshl_or_b32 v6, v228, 16, v224\n"
    "v_lshl_or_b32 v7, v229, 16, v225\n"
    "v_mfma_f32_16x16x32_bf16 v[108:111], v[12:15], v[212:215], v[108:111]\n"
    "ds_read_b128 v[12:15], v1 offset:10240\n"
    "s_barrier\n"
    "ds_read2st64_b64 v[232:235], v5 offset0:96 offset1:97\n"
    "ds_read2st64_b64 v[236:239], v5 offset0:98 offset1:99\n"
    "v_xor_b32_e32 v0, s74, v2\n"
    "v_xor_b32_e64 v4, s74, v3\n"
    "v_mfma_f32_16x16x32_bf16 v[48:51], v[32:35], v[216:219], v[48:51]\n"
    "v_perm_b32 v224, v17, v16, v2\n"
    "v_perm_b32 v228, v25, v24, v3\n"
    "v_perm_b32 v225, v21, v20, v2\n"
    "v_perm_b32 v229, v29, v28, v3\n"
    "v_mfma_f32_16x16x32_bf16 v[52:55], v[32:35], v[220:223], v[52:55]\n"
    "ds_read_b128 v[32:35], v1 offset:12288\n"
    "v_perm_b32 v2, v19, v18, v0\n"
    "v_perm_b32 v3, v27, v26, v4\n"
    "v_perm_b32 v0, v23, v22, v0\n"
    "v_perm_b32 v4, v31, v30, v4\n"
    "s_waitcnt lgkmcnt(7)\n"
    "v_mfma_f32_16x16x32_bf16 v[56:59], v[36:39], v[216:219], v[56:59]\n"
    "v_xor_b32_e32 v2, v224, v2\n"
    "v_xor_b32_e32 v3, v228, v3\n"
    "v_xor_b32_e32 v0, v225, v0\n"
    "v_xor_b32_e64 v4, v229, v4\n"
    "v_mfma_f32_16x16x32_bf16 v[60:63], v[36:39], v[220:223], v[60:63]\n"
    "ds_read_b128 v[36:39], v1 offset:14336\n"
    "v_perm_b32 v224, v0, v2, s75\n"
    "v_perm_b32 v228, v4, v3, s75\n"
    "v_perm_b32 v225, v0, v2, s76\n"
    "s_waitcnt lgkmcnt(7)\n"
    "v_mfma_f32_16x16x32_bf16 v[64:67], v[40:43], v[216:219], v[64:67]\n"
    "v_perm_b32 v229, v4, v3, s76\n"
    "s_waitcnt lgkmcnt(2)\n"
    "v_lshl_or_b32 v236, v238, 8, v236\n"
    "v_lshl_or_b32 v232, v234, 8, v232\n"
    "v_mfma_f32_16x16x32_bf16 v[68:71], v[40:43], v[220:223], v[68:71]\n"
    "v_xor_b32_e64 v1, 64, v1\n"
    "ds_read_b128 v[40:43], v1 offset:16384\n"
    "v_lshl_or_b32 v237, v239, 8, v237\n"
    "v_lshl_or_b32 v233, v235, 8, v233\n"
    "v_lshl_or_b32 v2, v236, 16, v232\n"
    "v_mfma_f32_16x16x32_bf16 v[72:75], v[44:47], v[216:219], v[72:75]\n"
    "v_lshl_or_b32 v3, v237, 16, v233\n"
    "ds_read2st64_b64 v[232:235], v5 offset0:100 offset1:101\n"
    "ds_read2st64_b64 v[236:239], v5 offset0:102 offset1:103\n"
    "v_mfma_f32_16x16x32_bf16 v[76:79], v[44:47], v[220:223], v[76:79]\n"
    "ds_read_b128 v[44:47], v1 offset:18432\n"
    "v_xor_b32_e32 v0, s74, v6\n"
    "v_xor_b32_e32 v4, s74, v7\n"
    "v_perm_b32 v226, v17, v16, v6\n"
    "v_mfma_f32_16x16x32_bf16 v[80:83], v[8:11], v[216:219], v[80:83]\n"
    "v_perm_b32 v230, v25, v24, v7\n"
    "v_perm_b32 v227, v21, v20, v6\n"
    "v_perm_b32 v231, v29, v28, v7\n"
    "v_mfma_f32_16x16x32_bf16 v[84:87], v[8:11], v[220:223], v[84:87]\n"
    "ds_read_b128 v[8:11], v1 offset:20480\n"
    "v_perm_b32 v6, v19, v18, v0\n"
    "v_perm_b32 v7, v27, v26, v4\n"
    "v_perm_b32 v0, v23, v22, v0\n"
    "v_mfma_f32_16x16x32_bf16 v[88:91], v[12:15], v[216:219], v[88:91]\n"
    "v_perm_b32 v4, v31, v30, v4\n"
    "v_xor_b32_e32 v6, v226, v6\n"
    "v_xor_b32_e32 v7, v230, v7\n"
    "v_mfma_f32_16x16x32_bf16 v[92:95], v[12:15], v[220:223], v[92:95]\n"
    "ds_read_b128 v[12:15], v1 offset:22528\n"
    "v_xor_b32_e32 v0, v227, v0\n"
    "v_xor_b32_e32 v4, v231, v4\n"
    "v_perm_b32 v226, v0, v6, s75\n"
    "s_waitcnt lgkmcnt(7)\n"
    "v_mfma_f32_16x16x32_bf16 v[96:99], v[32:35], v[216:219], v[96:99]\n"
    "v_perm_b32 v230, v4, v7, s75\n"
    "v_perm_b32 v227, v0, v6, s76\n"
    "v_perm_b32 v231, v4, v7, s76\n"
    "v_mfma_f32_16x16x32_bf16 v[100:103], v[32:35], v[220:223], v[100:103]\n"
    "ds_read_b128 v[32:35], v1 offset:24576\n"
    "s_waitcnt lgkmcnt(4)\n"
    "v_lshl_or_b32 v236, v238, 8, v236\n"
    "v_lshl_or_b32 v232, v234, 8, v232\n"
    "v_lshl_or_b32 v237, v239, 8, v237\n"
    "v_mfma_f32_16x16x32_bf16 v[104:107], v[36:39], v[216:219], v[104:107]\n"
    "v_lshl_or_b32 v233, v235, 8, v233\n"
    "v_lshl_or_b32 v6, v236, 16, v232\n"
    "v_lshl_or_b32 v7, v237, 16, v233\n"
    "v_mfma_f32_16x16x32_bf16 v[108:111], v[36:39], v[220:223], v[108:111]\n"
    "ds_read_b128 v[36:39], v1 offset:26624\n"
    "s_barrier\n"
    "ds_read2st64_b64 v[240:243], v5 offset0:0 offset1:1\n"
    "ds_read2st64_b64 v[244:247], v5 offset0:2 offset1:3\n"
    "v_xor_b32_e32 v0, s74, v2\n"
    "v_xor_b32_e64 v4, s74, v3\n"
    "v_mfma_f32_16x16x32_bf16 v[48:51], v[40:43], v[224:227], v[48:51]\n"
    "v_perm_b32 v232, v17, v16, v2\n"
    "v_perm_b32 v236, v25, v24, v3\n"
    "v_perm_b32 v233, v21, v20, v2\n"
    "v_perm_b32 v237, v29, v28, v3\n"
    "v_mfma_f32_16x16x32_bf16 v[52:55], v[40:43], v[228:231], v[52:55]\n"
    "ds_read_b128 v[40:43], v1 offset:28672\n"
    "v_perm_b32 v2, v19, v18, v0\n"
    "v_perm_b32 v3, v27, v26, v4\n"
    "v_perm_b32 v0, v23, v22, v0\n"
    "v_perm_b32 v4, v31, v30, v4\n"
    "s_waitcnt lgkmcnt(7)\n"
    "v_mfma_f32_16x16x32_bf16 v[56:59], v[44:47], v[224:227], v[56:59]\n"
    "v_xor_b32_e32 v2, v232, v2\n"
    "v_xor_b32_e32 v3, v236, v3\n"
    "v_xor_b32_e32 v0, v233, v0\n"
    "v_xor_b32_e64 v4, v237, v4\n"
    "v_mfma_f32_16x16x32_bf16 v[60:63], v[44:47], v[228:231], v[60:63]\n"
    "ds_read_b128 v[44:47], v1 offset:30720\n"
    "v_perm_b32 v232, v0, v2, s75\n"
    "v_perm_b32 v236, v4, v3, s75\n"
    "v_perm_b32 v233, v0, v2, s76\n"
    "s_waitcnt lgkmcnt(7)\n"
    "v_mfma_f32_16x16x32_bf16 v[64:67], v[8:11], v[224:227], v[64:67]\n"
    "v_perm_b32 v237, v4, v3, s76\n"
    "s_waitcnt lgkmcnt(2)\n"
    "v_lshl_or_b32 v244, v246, 8, v244\n"
    "v_lshl_or_b32 v240, v242, 8, v240\n"
    "v_mfma_f32_16x16x32_bf16 v[68:71], v[8:11], v[228:231], v[68:71]\n"
    "v_xor_b32_e64 v1, 64, v1\n"
    "ds_read_b128 v[8:11], v1 offset:16384\n"
    "v_lshl_or_b32 v245, v247, 8, v245\n"
    "v_lshl_or_b32 v241, v243, 8, v241\n"
    "v_lshl_or_b32 v2, v244, 16, v240\n"
    "v_mfma_f32_16x16x32_bf16 v[72:75], v[12:15], v[224:227], v[72:75]\n"
    "v_lshl_or_b32 v3, v245, 16, v241\n"
    "ds_read2st64_b64 v[240:243], v5 offset0:4 offset1:5\n"
    "ds_read2st64_b64 v[244:247], v5 offset0:6 offset1:7\n"
    "v_mfma_f32_16x16x32_bf16 v[76:79], v[12:15], v[228:231], v[76:79]\n"
    "ds_read_b128 v[12:15], v1 offset:18432\n"
    "v_xor_b32_e32 v0, s74, v6\n"
    "v_xor_b32_e32 v4, s74, v7\n"
    "v_perm_b32 v234, v17, v16, v6\n"
    "v_mfma_f32_16x16x32_bf16 v[80:83], v[32:35], v[224:227], v[80:83]\n"
    "v_perm_b32 v238, v25, v24, v7\n"
    "v_perm_b32 v235, v21, v20, v6\n"
    "v_perm_b32 v239, v29, v28, v7\n"
    "v_mfma_f32_16x16x32_bf16 v[84:87], v[32:35], v[228:231], v[84:87]\n"
    "ds_read_b128 v[32:35], v1 offset:20480\n"
    "v_perm_b32 v6, v19, v18, v0\n"
    "v_perm_b32 v7, v27, v26, v4\n"
    "v_perm_b32 v0, v23, v22, v0\n"
    "v_mfma_f32_16x16x32_bf16 v[88:91], v[36:39], v[224:227], v[88:91]\n"
    "v_perm_b32 v4, v31, v30, v4\n"
    "v_xor_b32_e32 v6, v234, v6\n"
    "v_xor_b32_e32 v7, v238, v7\n"
    "v_mfma_f32_16x16x32_bf16 v[92:95], v[36:39], v[228:231], v[92:95]\n"
    "ds_read_b128 v[36:39], v1 offset:22528\n"
    "v_xor_b32_e32 v0, v235, v0\n"
    "v_xor_b32_e32 v4, v239, v4\n"
    "v_perm_b32 v234, v0, v6, s75\n"
    "s_waitcnt lgkmcnt(7)\n"
    "v_mfma_f32_16x16x32_bf16 v[96:99], v[40:43], v[224:227], v[96:99]\n"
    "v_perm_b32 v238, v4, v7, s75\n"
    "v_perm_b32 v235, v0, v6, s76\n"
    "v_perm_b32 v239, v4, v7, s76\n"
    "v_mfma_f32_16x16x32_bf16 v[100:103], v[40:43], v[228:231], v[100:103]\n"
    "ds_read_b128 v[40:43], v1 offset:24576\n"
    "s_waitcnt lgkmcnt(4)\n"
    "v_lshl_or_b32 v244, v246, 8, v244\n"
    "v_lshl_or_b32 v240, v242, 8, v240\n"
    "v_lshl_or_b32 v245, v247, 8, v245\n"
    "v_mfma_f32_16x16x32_bf16 v[104:107], v[44:47], v[224:227], v[104:107]\n"
    "v_lshl_or_b32 v241, v243, 8, v241\n"
    "v_lshl_or_b32 v6, v244, 16, v240\n"
    "v_lshl_or_b32 v7, v245, 16, v241\n"
    "v_mfma_f32_16x16x32_bf16 v[108:111], v[44:47], v[228:231], v[108:111]\n"
    "ds_read_b128 v[44:47], v1 offset:26624\n"
    "s_barrier\n"
    "ds_read2st64_b64 v[248:251], v5 offset0:32 offset1:33\n"
    "ds_read2st64_b64 v[252:255], v5 offset0:34 offset1:35\n"
    "v_xor_b32_e32 v0, s74, v2\n"
    "v_xor_b32_e64 v4, s74, v3\n"
    "v_mfma_f32_16x16x32_bf16 v[48:51], v[8:11], v[232:235], v[48:51]\n"
    "v_perm_b32 v240, v17, v16, v2\n"
    "v_perm_b32 v244, v25, v24, v3\n"
    "v_perm_b32 v241, v21, v20, v2\n"
    "v_perm_b32 v245, v29, v28, v3\n"
    "v_mfma_f32_16x16x32_bf16 v[52:55], v[8:11], v[236:239], v[52:55]\n"
    "ds_read_b128 v[8:11], v1 offset:28672\n"
    "v_perm_b32 v2, v19, v18, v0\n"
    "v_perm_b32 v3, v27, v26, v4\n"
    "v_perm_b32 v0, v23, v22, v0\n"
    "v_perm_b32 v4, v31, v30, v4\n"
    "s_waitcnt lgkmcnt(7)\n"
    "v_mfma_f32_16x16x32_bf16 v[56:59], v[12:15], v[232:235], v[56:59]\n"
    "v_xor_b32_e32 v2, v240, v2\n"
    "v_xor_b32_e32 v3, v244, v3\n"
    "v_xor_b32_e32 v0, v241, v0\n"
    "v_xor_b32_e64 v4, v245, v4\n"
    "v_mfma_f32_16x16x32_bf16 v[60:63], v[12:15], v[236:239], v[60:63]\n"
    "ds_read_b128 v[12:15], v1 offset:30720\n"
    "v_perm_b32 v240, v0, v2, s75\n"
    "v_perm_b32 v244, v4, v3, s75\n"
    "v_perm_b32 v241, v0, v2, s76\n"
    "s_waitcnt lgkmcnt(7)\n"
    "v_mfma_f32_16x16x32_bf16 v[64:67], v[32:35], v[232:235], v[64:67]\n"
    "v_perm_b32 v245, v4, v3, s76\n"
    "s_waitcnt lgkmcnt(2)\n"
    "v_lshl_or_b32 v252, v254, 8, v252\n"
    "v_lshl_or_b32 v248, v250, 8, v248\n"
    "v_mfma_f32_16x16x32_bf16 v[68:71], v[32:35], v[236:239], v[68:71]\n"
    "v_xor_b32_e64 v1, 64, v1\n"
    "ds_read_b128 v[32:35], v1 offset:32768\n"
    "v_lshl_or_b32 v253, v255, 8, v253\n"
    "v_lshl_or_b32 v249, v251, 8, v249\n"
    "v_lshl_or_b32 v2, v252, 16, v248\n"
    "v_mfma_f32_16x16x32_bf16 v[72:75], v[36:39], v[232:235], v[72:75]\n"
    "v_lshl_or_b32 v3, v253, 16, v249\n"
    "ds_read2st64_b64 v[248:251], v5 offset0:36 offset1:37\n"
    "ds_read2st64_b64 v[252:255], v5 offset0:38 offset1:39\n"
    "v_mfma_f32_16x16x32_bf16 v[76:79], v[36:39], v[236:239], v[76:79]\n"
    "ds_read_b128 v[36:39], v1 offset:34816\n"
    "v_xor_b32_e32 v0, s74, v6\n"
    "v_xor_b32_e32 v4, s74, v7\n"
    "v_perm_b32 v242, v17, v16, v6\n"
    "v_mfma_f32_16x16x32_bf16 v[80:83], v[40:43], v[232:235], v[80:83]\n"
    "v_perm_b32 v246, v25, v24, v7\n"
    "v_perm_b32 v243, v21, v20, v6\n"
    "v_perm_b32 v247, v29, v28, v7\n"
    "v_mfma_f32_16x16x32_bf16 v[84:87], v[40:43], v[236:239], v[84:87]\n"
    "ds_read_b128 v[40:43], v1 offset:36864\n"
    "v_perm_b32 v6, v19, v18, v0\n"
    "v_perm_b32 v7, v27, v26, v4\n"
    "v_perm_b32 v0, v23, v22, v0\n"
    "v_mfma_f32_16x16x32_bf16 v[88:91], v[44:47], v[232:235], v[88:91]\n"
    "v_perm_b32 v4, v31, v30, v4\n"
    "v_xor_b32_e32 v6, v242, v6\n"
    "v_xor_b32_e32 v7, v246, v7\n"
    "v_mfma_f32_16x16x32_bf16 v[92:95], v[44:47], v[236:239], v[92:95]\n"
    "ds_read_b128 v[44:47], v1 offset:38912\n"
    "v_xor_b32_e32 v0, v243, v0\n"
    "v_xor_b32_e32 v4, v247, v4\n"
    "v_perm_b32 v242, v0, v6, s75\n"
    "s_waitcnt lgkmcnt(7)\n"
    "v_mfma_f32_16x16x32_bf16 v[96:99], v[8:11], v[232:235], v[96:99]\n"
    "v_perm_b32 v246, v4, v7, s75\n"
    "v_perm_b32 v243, v0, v6, s76\n"
    "v_perm_b32 v247, v4, v7, s76\n"
    "v_mfma_f32_16x16x32_bf16 v[100:103], v[8:11], v[236:239], v[100:103]\n"
    "ds_read_b128 v[8:11], v1 offset:40960\n"
    "s_waitcnt lgkmcnt(4)\n"
    "v_lshl_or_b32 v252, v254, 8, v252\n"
    "v_lshl_or_b32 v248, v250, 8, v248\n"
    "v_lshl_or_b32 v253, v255, 8, v253\n"
    "v_mfma_f32_16x16x32_bf16 v[104:107], v[12:15], v[232:235], v[104:107]\n"
    "v_lshl_or_b32 v249, v251, 8, v249\n"
    "v_lshl_or_b32 v6, v252, 16, v248\n"
    "v_lshl_or_b32 v7, v253, 16, v249\n"
    "v_mfma_f32_16x16x32_bf16 v[108:111], v[12:15], v[236:239], v[108:111]\n"
    "ds_read_b128 v[12:15], v1 offset:43008\n"
    "s_barrier\n"
    "v_xor_b32_e32 v0, s74, v2\n"
    "v_xor_b32_e64 v4, s74, v3\n"
    "v_perm_b32 v248, v17, v16, v2\n"
    "v_mfma_f32_16x16x32_bf16 v[48:51], v[32:35], v[240:243], v[48:51]\n"
    "v_perm_b32 v252, v25, v24, v3\n"
    "v_perm_b32 v249, v21, v20, v2\n"
    "v_perm_b32 v253, v29, v28, v3\n"
    "v_mfma_f32_16x16x32_bf16 v[52:55], v[32:35], v[244:247], v[52:55]\n"
    "ds_read_b128 v[32:35], v1 offset:45056\n"
    "v_perm_b32 v2, v19, v18, v0\n"
    "v_perm_b32 v3, v27, v26, v4\n"
    "v_perm_b32 v0, v23, v22, v0\n"
    "s_waitcnt lgkmcnt(5)\n"
    "v_mfma_f32_16x16x32_bf16 v[56:59], v[36:39], v[240:243], v[56:59]\n"
    "v_perm_b32 v4, v31, v30, v4\n"
    "v_xor_b32_e32 v2, v248, v2\n"
    "v_xor_b32_e64 v3, v252, v3\n"
    "v_mfma_f32_16x16x32_bf16 v[60:63], v[36:39], v[244:247], v[60:63]\n"
    "ds_read_b128 v[36:39], v1 offset:47104\n"
    "v_xor_b32_e32 v0, v249, v0\n"
    "v_xor_b32_e32 v4, v253, v4\n"
    "s_waitcnt lgkmcnt(5)\n"
    "v_mfma_f32_16x16x32_bf16 v[64:67], v[40:43], v[240:243], v[64:67]\n"
    "v_perm_b32 v248, v0, v2, s75\n"
    "v_perm_b32 v252, v4, v3, s75\n"
    "v_mfma_f32_16x16x32_bf16 v[68:71], v[40:43], v[244:247], v[68:71]\n"
    "v_xor_b32_e32 v1, 64, v1\n"
    "ds_read_b128 v[40:43], v1 offset:32768\n"
    "v_perm_b32 v249, v0, v2, s76\n"
    "v_perm_b32 v253, v4, v3, s76\n"
    "s_waitcnt lgkmcnt(5)\n"
    "v_mfma_f32_16x16x32_bf16 v[72:75], v[44:47], v[240:243], v[72:75]\n"
    "v_xor_b32_e32 v0, s74, v6\n"
    "v_xor_b32_e64 v4, s74, v7\n"
    "v_mfma_f32_16x16x32_bf16 v[76:79], v[44:47], v[244:247], v[76:79]\n"
    "ds_read_b128 v[44:47], v1 offset:34816\n"
    "v_perm_b32 v250, v17, v16, v6\n"
    "v_perm_b32 v254, v25, v24, v7\n"
    "s_waitcnt lgkmcnt(5)\n"
    "v_mfma_f32_16x16x32_bf16 v[80:83], v[8:11], v[240:243], v[80:83]\n"
    "v_perm_b32 v251, v21, v20, v6\n"
    "v_perm_b32 v255, v29, v28, v7\n"
    "v_mfma_f32_16x16x32_bf16 v[84:87], v[8:11], v[244:247], v[84:87]\n"
    "ds_read_b128 v[8:11], v1 offset:36864\n"
    "v_perm_b32 v6, v19, v18, v0\n"
    "v_perm_b32 v7, v27, v26, v4\n"
    "s_waitcnt lgkmcnt(5)\n"
    "v_mfma_f32_16x16x32_bf16 v[88:91], v[12:15], v[240:243], v[88:91]\n"
    "v_perm_b32 v0, v23, v22, v0\n"
    "v_perm_b32 v4, v31, v30, v4\n"
    "v_mfma_f32_16x16x32_bf16 v[92:95], v[12:15], v[244:247], v[92:95]\n"
    "ds_read_b128 v[12:15], v1 offset:38912\n"
    "v_xor_b32_e32 v6, v250, v6\n"
    "v_xor_b32_e32 v7, v254, v7\n"
    "s_waitcnt lgkmcnt(5)\n"
    "v_mfma_f32_16x16x32_bf16 v[96:99], v[32:35], v[240:243], v[96:99]\n"
    "v_xor_b32_e32 v0, v251, v0\n"
    "v_xor_b32_e64 v4, v255, v4\n"
    "v_mfma_f32_16x16x32_bf16 v[100:103], v[32:35], v[244:247], v[100:103]\n"
    "ds_read_b128 v[32:35], v1 offset:40960\n"
    "v_perm_b32 v250, v0, v6, s75\n"
    "v_perm_b32 v254, v4, v7, s75\n"
    "s_waitcnt lgkmcnt(5)\n"
    "v_mfma_f32_16x16x32_bf16 v[104:107], v[36:39], v[240:243], v[104:107]\n"
    "v_perm_b32 v251, v0, v6, s76\n"
    "v_perm_b32 v255, v4, v7, s76\n"
    "v_mfma_f32_16x16x32_bf16 v[108:111], v[36:39], v[244:247], v[108:111]\n"
    "ds_read_b128 v[36:39], v1 offset:43008\n"
    "s_barrier\n"
    "ds_read_b128 v[20:23], v1 offset:45056\n"
    "s_waitcnt lgkmcnt(6)\n"
    "v_mfma_f32_16x16x32_bf16 v[48:51], v[40:43], v[248:251], v[48:51]\n"
    "v_add_u32_e32 v7, 4294852608, v1\n"
    "v_add_u32_e32 v0, 4294918144, v1\n"
    "v_mfma_f32_16x16x32_bf16 v[52:55], v[40:43], v[252:255], v[52:55]\n"
    "ds_read_b128 v[40:43], v1 offset:47104\n"
    "s_waitcnt lgkmcnt(6)\n"
    "v_mfma_f32_16x16x32_bf16 v[56:59], v[44:47], v[248:251], v[56:59]\n"
    "v_add_u32_e32 v4, 16384, v1\n"
    "v_xor_b32_e64 v2, 64, v7\n"
    "v_mfma_f32_16x16x32_bf16 v[60:63], v[44:47], v[252:255], v[60:63]\n"
    "s_waitcnt lgkmcnt(5)\n"
    "v_mfma_f32_16x16x32_bf16 v[64:67], v[8:11], v[248:251], v[64:67]\n"
    "v_xor_b32_e32 v3, 64, v0\n"
    "v_xor_b32_e64 v6, 64, v4\n"
    "v_mfma_f32_16x16x32_bf16 v[68:71], v[8:11], v[252:255], v[68:71]\n"
    "s_waitcnt lgkmcnt(4)\n"
    "v_mfma_f32_16x16x32_bf16 v[72:75], v[12:15], v[248:251], v[72:75]\n"
    "v_cvt_pk_bf16_f32 v48, v48, v52\n"
    "v_cvt_pk_bf16_f32 v49, v49, v53\n"
    "v_mfma_f32_16x16x32_bf16 v[76:79], v[12:15], v[252:255], v[76:79]\n"
    "s_waitcnt lgkmcnt(3)\n"
    "v_mfma_f32_16x16x32_bf16 v[80:83], v[32:35], v[248:251], v[80:83]\n"
    "v_cvt_pk_bf16_f32 v50, v50, v54\n"
    "v_cvt_pk_bf16_f32 v51, v51, v55\n"
    "v_mfma_f32_16x16x32_bf16 v[84:87], v[32:35], v[252:255], v[84:87]\n"
    "s_waitcnt lgkmcnt(2)\n"
    "v_mfma_f32_16x16x32_bf16 v[88:91], v[36:39], v[248:251], v[88:91]\n"
    "v_cvt_pk_bf16_f32 v56, v56, v60\n"
    "v_cvt_pk_bf16_f32 v57, v57, v61\n"
    "v_mfma_f32_16x16x32_bf16 v[92:95], v[36:39], v[252:255], v[92:95]\n"
    "s_waitcnt lgkmcnt(1)\n"
    "v_mfma_f32_16x16x32_bf16 v[96:99], v[20:23], v[248:251], v[96:99]\n"
    "v_cvt_pk_bf16_f32 v58, v58, v62\n"
    "v_cvt_pk_bf16_f32 v59, v59, v63\n"
    "s_barrier\n"
    "ds_read_b128 v[44:47], v3 offset:0\n"
    "ds_read_b128 v[8:11], v0 offset:0\n"
    "v_mfma_f32_16x16x32_bf16 v[100:103], v[20:23], v[252:255], v[100:103]\n"
    "ds_read_b128 v[12:15], v3 offset:2048\n"
    "s_waitcnt lgkmcnt(3)\n"
    "v_mfma_f32_16x16x32_bf16 v[104:107], v[40:43], v[248:251], v[104:107]\n"
    "s_mov_b32 s77, 0\n"
    "s_mov_b32 s78, 147456\n"
    "ds_read_b128 v[32:35], v0 offset:2048\n"
    "v_mfma_f32_16x16x32_bf16 v[108:111], v[40:43], v[252:255], v[108:111]\n"
    "ds_read_b128 v[36:39], v3 offset:4096\n"
    "s_waitcnt lgkmcnt(4)\n"
    "v_mfma_f32_16x16x32_bf16 v[52:55], v[44:47], v[112:115], 0\n"
    "v_mbcnt_lo_u32_b32 v17, -1, 0\n"
    "v_mbcnt_hi_u32_b32 v17, -1, v17\n"
    "ds_read_b128 v[20:23], v0 offset:4096\n"
    "v_mfma_f32_16x16x32_bf16 v[60:63], v[44:47], v[116:119], 0\n"
    "ds_read_b128 v[40:43], v3 offset:6144\n"
    "s_waitcnt lgkmcnt(5)\n"
    "v_mfma_f32_16x16x32_bf16 v[52:55], v[8:11], v[120:123], v[52:55]\n"
    "v_lshrrev_b32_e32 v18, 4, v17\n"
    "v_and_b32_e64 v17, 15, v17\n"
    "ds_read_b128 v[44:47], v0 offset:6144\n"
    "v_mfma_f32_16x16x32_bf16 v[60:63], v[8:11], v[124:127], v[60:63]\n"
    "ds_read_b128 v[8:11], v3 offset:8192\n"
    "s_waitcnt lgkmcnt(6)\n"
    "v_mfma_f32_16x16x32_bf16 v[52:55], v[12:15], v[128:131], v[52:55]\n"
    "v_mul_u32_u24_e32 v16, 36864, v18\n"
    "v_lshl_add_u32 v16, v17, 2, v16\n"
    "v_mfma_f32_16x16x32_bf16 v[60:63], v[12:15], v[132:135], v[60:63]\n"
    "ds_read_b128 v[12:15], v0 offset:8192\n"
    "s_waitcnt lgkmcnt(6)\n"
    "v_mfma_f32_16x16x32_bf16 v[52:55], v[32:35], v[136:139], v[52:55]\n"
    "v_add_u32_e64 v16, s51, v16\n"
    "v_add_u32_e32 v17, 9216, v16\n"
    "v_mfma_f32_16x16x32_bf16 v[60:63], v[32:35], v[140:143], v[60:63]\n"
    "ds_read_b128 v[32:35], v3 offset:10240\n"
    "s_waitcnt lgkmcnt(6)\n"
    "v_mfma_f32_16x16x32_bf16 v[52:55], v[36:39], v[144:147], v[52:55]\n"
    "v_add_u32_e32 v18, 18432, v16\n"
    "v_add_u32_e32 v19, 27648, v16\n"
    "v_mfma_f32_16x16x32_bf16 v[60:63], v[36:39], v[148:151], v[60:63]\n"
    "ds_read_b128 v[36:39], v0 offset:10240\n"
    "s_waitcnt lgkmcnt(6)\n"
    "v_mfma_f32_16x16x32_bf16 v[52:55], v[20:23], v[152:155], v[52:55]\n"
    "v_cvt_pk_bf16_f32 v64, v64, v68\n"
    "v_cvt_pk_bf16_f32 v65, v65, v69\n"
    "v_mfma_f32_16x16x32_bf16 v[60:63], v[20:23], v[156:159], v[60:63]\n"
    "ds_read_b128 v[20:23], v3 offset:12288\n"
    "s_waitcnt lgkmcnt(6)\n"
    "v_mfma_f32_16x16x32_bf16 v[52:55], v[40:43], v[160:163], v[52:55]\n"
    "v_cvt_pk_bf16_f32 v66, v66, v70\n"
    "v_cvt_pk_bf16_f32 v67, v67, v71\n"
    "v_mfma_f32_16x16x32_bf16 v[60:63], v[40:43], v[164:167], v[60:63]\n"
    "ds_read_b128 v[40:43], v0 offset:12288\n"
    "s_waitcnt lgkmcnt(6)\n"
    "v_mfma_f32_16x16x32_bf16 v[52:55], v[44:47], v[168:171], v[52:55]\n"
    "v_cvt_pk_bf16_f32 v72, v72, v76\n"
    "v_cvt_pk_bf16_f32 v73, v73, v77\n"
    "v_mfma_f32_16x16x32_bf16 v[60:63], v[44:47], v[172:175], v[60:63]\n"
    "ds_read_b128 v[44:47], v3 offset:14336\n"
    "s_waitcnt lgkmcnt(6)\n"
    "v_mfma_f32_16x16x32_bf16 v[52:55], v[8:11], v[176:179], v[52:55]\n"
    "v_cvt_pk_bf16_f32 v74, v74, v78\n"
    "v_cvt_pk_bf16_f32 v75, v75, v79\n"
    "v_mfma_f32_16x16x32_bf16 v[60:63], v[8:11], v[180:183], v[60:63]\n"
    "ds_read_b128 v[8:11], v0 offset:14336\n"
    "s_waitcnt lgkmcnt(6)\n"
    "v_mfma_f32_16x16x32_bf16 v[52:55], v[12:15], v[184:187], v[52:55]\n"
    "buffer_store_dword v48, v16, s[28:31], s77 offen nt\n"
    "v_mfma_f32_16x16x32_bf16 v[60:63], v[12:15], v[188:191], v[60:63]\n"
    "ds_read_b128 v[12:15], v3 offset:16384\n"
    "s_waitcnt lgkmcnt(6)\n"
    "v_mfma_f32_16x16x32_bf16 v[52:55], v[32:35], v[192:195], v[52:55]\n"
    "buffer_store_dword v49, v17, s[28:31], s77 offen nt\n"
    "v_mfma_f32_16x16x32_bf16 v[60:63], v[32:35], v[196:199], v[60:63]\n"
    "ds_read_b128 v[32:35], v0 offset:16384\n"
    "s_waitcnt lgkmcnt(6)\n"
    "v_mfma_f32_16x16x32_bf16 v[52:55], v[36:39], v[200:203], v[52:55]\n"
    "buffer_store_dword v50, v18, s[28:31], s77 offen nt\n"
    "v_mfma_f32_16x16x32_bf16 v[60:63], v[36:39], v[204:207], v[60:63]\n"
    "s_waitcnt lgkmcnt(5)\n"
    "v_mfma_f32_16x16x32_bf16 v[52:55], v[20:23], v[208:211], v[52:55]\n"
    "buffer_store_dword v51, v19, s[28:31], s77 offen nt\n"
    "v_mfma_f32_16x16x32_bf16 v[60:63], v[20:23], v[212:215], v[60:63]\n"
    "s_waitcnt lgkmcnt(4)\n"
    "v_mfma_f32_16x16x32_bf16 v[52:55], v[40:43], v[216:219], v[52:55]\n"
    "buffer_store_dword v56, v16, s[28:31], s78 offen nt\n"
    "v_mfma_f32_16x16x32_bf16 v[60:63], v[40:43], v[220:223], v[60:63]\n"
    "s_waitcnt lgkmcnt(3)\n"
    "v_mfma_f32_16x16x32_bf16 v[52:55], v[44:47], v[224:227], v[52:55]\n"
    "buffer_store_dword v57, v17, s[28:31], s78 offen nt\n"
    "v_mfma_f32_16x16x32_bf16 v[60:63], v[44:47], v[228:231], v[60:63]\n"
    "s_waitcnt lgkmcnt(2)\n"
    "v_mfma_f32_16x16x32_bf16 v[52:55], v[8:11], v[232:235], v[52:55]\n"
    "buffer_store_dword v58, v18, s[28:31], s78 offen nt\n"
    "v_mfma_f32_16x16x32_bf16 v[60:63], v[8:11], v[236:239], v[60:63]\n"
    "s_waitcnt lgkmcnt(1)\n"
    "v_mfma_f32_16x16x32_bf16 v[52:55], v[12:15], v[240:243], v[52:55]\n"
    "buffer_store_dword v59, v19, s[28:31], s78 offen nt\n"
    "s_barrier\n"
    "ds_read_b128 v[36:39], v3 offset:18432\n"
    "ds_read_b128 v[20:23], v0 offset:18432\n"
    "v_mfma_f32_16x16x32_bf16 v[60:63], v[12:15], v[244:247], v[60:63]\n"
    "ds_read_b128 v[40:43], v3 offset:20480\n"
    "s_waitcnt lgkmcnt(3)\n"
    "v_mfma_f32_16x16x32_bf16 v[52:55], v[32:35], v[248:251], v[52:55]\n"
    "s_lshl_b32 s79, s58, 3\n"
    "ds_read_b128 v[44:47], v0 offset:20480\n"
    "v_mfma_f32_16x16x32_bf16 v[60:63], v[32:35], v[252:255], v[60:63]\n"
    "ds_read_b128 v[8:11], v3 offset:22528\n"
    "s_waitcnt lgkmcnt(4)\n"
    "v_mfma_f32_16x16x32_bf16 v[68:71], v[36:39], v[112:115], 0\n"
    "v_cvt_pk_bf16_f32 v80, v80, v84\n"
    "v_cvt_pk_bf16_f32 v81, v81, v85\n"
    "ds_read_b128 v[12:15], v0 offset:22528\n"
    "v_mfma_f32_16x16x32_bf16 v[76:79], v[36:39], v[116:119], 0\n"
    "ds_read_b128 v[32:35], v3 offset:24576\n"
    "s_waitcnt lgkmcnt(5)\n"
    "v_mfma_f32_16x16x32_bf16 v[68:71], v[20:23], v[120:123], v[68:71]\n"
    "v_cvt_pk_bf16_f32 v82, v82, v86\n"
    "v_cvt_pk_bf16_f32 v83, v83, v87\n"
    "ds_read_b128 v[36:39], v0 offset:24576\n"
    "v_mfma_f32_16x16x32_bf16 v[76:79], v[20:23], v[124:127], v[76:79]\n"
    "ds_read_b128 v[20:23], v3 offset:26624\n"
    "s_waitcnt lgkmcnt(6)\n"
    "v_mfma_f32_16x16x32_bf16 v[68:71], v[40:43], v[128:131], v[68:71]\n"
    "v_cvt_pk_bf16_f32 v88, v88, v92\n"
    "v_cvt_pk_bf16_f32 v89, v89, v93\n"
    "v_mfma_f32_16x16x32_bf16 v[76:79], v[40:43], v[132:135], v[76:79]\n"
    "ds_read_b128 v[40:43], v0 offset:26624\n"
    "s_waitcnt lgkmcnt(6)\n"
    "v_mfma_f32_16x16x32_bf16 v[68:71], v[44:47], v[136:139], v[68:71]\n"
    "v_cvt_pk_bf16_f32 v90, v90, v94\n"
    "v_cvt_pk_bf16_f32 v91, v91, v95\n"
    "v_mfma_f32_16x16x32_bf16 v[76:79], v[44:47], v[140:143], v[76:79]\n"
    "ds_read_b128 v[44:47], v3 offset:28672\n"
    "s_waitcnt lgkmcnt(6)\n"
    "v_mfma_f32_16x16x32_bf16 v[68:71], v[8:11], v[144:147], v[68:71]\n"
    "v_cvt_pk_bf16_f32 v96, v96, v100\n"
    "v_cvt_pk_bf16_f32 v97, v97, v101\n"
    "v_mfma_f32_16x16x32_bf16 v[76:79], v[8:11], v[148:151], v[76:79]\n"
    "ds_read_b128 v[8:11], v0 offset:28672\n"
    "s_waitcnt lgkmcnt(6)\n"
    "v_mfma_f32_16x16x32_bf16 v[68:71], v[12:15], v[152:155], v[68:71]\n"
    "v_cvt_pk_bf16_f32 v98, v98, v102\n"
    "v_cvt_pk_bf16_f32 v99, v99, v103\n"
    "v_mfma_f32_16x16x32_bf16 v[76:79], v[12:15], v[156:159], v[76:79]\n"
    "ds_read_b128 v[12:15], v3 offset:30720\n"
    "s_waitcnt lgkmcnt(6)\n"
    "v_mfma_f32_16x16x32_bf16 v[68:71], v[32:35], v[160:163], v[68:71]\n"
    "v_cvt_pk_bf16_f32 v104, v104, v108\n"
    "v_cvt_pk_bf16_f32 v105, v105, v109\n"
    "v_mfma_f32_16x16x32_bf16 v[76:79], v[32:35], v[164:167], v[76:79]\n"
    "ds_read_b128 v[32:35], v0 offset:30720\n"
    "s_waitcnt lgkmcnt(6)\n"
    "v_mfma_f32_16x16x32_bf16 v[68:71], v[36:39], v[168:171], v[68:71]\n"
    "v_cvt_pk_bf16_f32 v106, v106, v110\n"
    "v_cvt_pk_bf16_f32 v107, v107, v111\n"
    "v_mfma_f32_16x16x32_bf16 v[76:79], v[36:39], v[172:175], v[76:79]\n"
    "ds_read_b128 v[36:39], v3 offset:32768\n"
    "s_waitcnt lgkmcnt(6)\n"
    "v_mfma_f32_16x16x32_bf16 v[68:71], v[20:23], v[176:179], v[68:71]\n"
    "v_cvt_pk_bf16_f32 v52, v52, v60\n"
    "v_cvt_pk_bf16_f32 v53, v53, v61\n"
    "v_mfma_f32_16x16x32_bf16 v[76:79], v[20:23], v[180:183], v[76:79]\n"
    "ds_read_b128 v[20:23], v0 offset:32768\n"
    "s_waitcnt lgkmcnt(6)\n"
    "v_mfma_f32_16x16x32_bf16 v[68:71], v[40:43], v[184:187], v[68:71]\n"
    "v_cvt_pk_bf16_f32 v54, v54, v62\n"
    "v_cvt_pk_bf16_f32 v55, v55, v63\n"
    "v_mfma_f32_16x16x32_bf16 v[76:79], v[40:43], v[188:191], v[76:79]\n"
    "ds_read_b128 v[40:43], v3 offset:34816\n"
    "s_waitcnt lgkmcnt(6)\n"
    "v_mfma_f32_16x16x32_bf16 v[68:71], v[44:47], v[192:195], v[68:71]\n"
    "buffer_store_dword v52, v16, s[28:31], s79 offen nt\n"
    "v_mfma_f32_16x16x32_bf16 v[76:79], v[44:47], v[196:199], v[76:79]\n"
    "ds_read_b128 v[44:47], v0 offset:34816\n"
    "s_waitcnt lgkmcnt(6)\n"
    "v_mfma_f32_16x16x32_bf16 v[68:71], v[8:11], v[200:203], v[68:71]\n"
    "buffer_store_dword v53, v17, s[28:31], s79 offen nt\n"
    "v_mfma_f32_16x16x32_bf16 v[76:79], v[8:11], v[204:207], v[76:79]\n"
    "s_waitcnt lgkmcnt(5)\n"
    "v_mfma_f32_16x16x32_bf16 v[68:71], v[12:15], v[208:211], v[68:71]\n"
    "buffer_store_dword v54, v18, s[28:31], s79 offen nt\n"
    "v_mfma_f32_16x16x32_bf16 v[76:79], v[12:15], v[212:215], v[76:79]\n"
    "s_waitcnt lgkmcnt(4)\n"
    "v_mfma_f32_16x16x32_bf16 v[68:71], v[32:35], v[216:219], v[68:71]\n"
    "buffer_store_dword v55, v19, s[28:31], s79 offen nt\n"
    "s_barrier\n"
    "ds_read_b128 v[8:11], v3 offset:36864\n"
    "v_mfma_f32_16x16x32_bf16 v[76:79], v[32:35], v[220:223], v[76:79]\n"
    "ds_read_b128 v[12:15], v0 offset:36864\n"
    "s_waitcnt lgkmcnt(5)\n"
    "v_mfma_f32_16x16x32_bf16 v[68:71], v[36:39], v[224:227], v[68:71]\n"
    "s_lshl_b32 s80, s59, 3\n"
    "ds_read_b128 v[32:35], v3 offset:38912\n"
    "v_mfma_f32_16x16x32_bf16 v[76:79], v[36:39], v[228:231], v[76:79]\n"
    "ds_read_b128 v[36:39], v0 offset:38912\n"
    "s_waitcnt lgkmcnt(6)\n"
    "v_mfma_f32_16x16x32_bf16 v[68:71], v[20:23], v[232:235], v[68:71]\n"
    "v_mfma_f32_16x16x32_bf16 v[76:79], v[20:23], v[236:239], v[76:79]\n"
    "ds_read_b128 v[20:23], v3 offset:40960\n"
    "s_waitcnt lgkmcnt(6)\n"
    "v_mfma_f32_16x16x32_bf16 v[68:71], v[40:43], v[240:243], v[68:71]\n"
    "v_mfma_f32_16x16x32_bf16 v[76:79], v[40:43], v[244:247], v[76:79]\n"
    "ds_read_b128 v[40:43], v0 offset:40960\n"
    "s_waitcnt lgkmcnt(6)\n"
    "v_mfma_f32_16x16x32_bf16 v[68:71], v[44:47], v[248:251], v[68:71]\n"
    "v_mfma_f32_16x16x32_bf16 v[76:79], v[44:47], v[252:255], v[76:79]\n"
    "ds_read_b128 v[44:47], v3 offset:43008\n"
    "s_waitcnt lgkmcnt(6)\n"
    "v_mfma_f32_16x16x32_bf16 v[52:55], v[8:11], v[112:115], 0\n"
    "v_mfma_f32_16x16x32_bf16 v[60:63], v[8:11], v[116:119], 0\n"
    "ds_read_b128 v[8:11], v0 offset:43008\n"
    "s_waitcnt lgkmcnt(6)\n"
    "v_mfma_f32_16x16x32_bf16 v[52:55], v[12:15], v[120:123], v[52:55]\n"
    "s_nop 0\n"
    "v_cvt_pk_bf16_f32 v68, v68, v76\n"
    "v_cvt_pk_bf16_f32 v69, v69, v77\n"
    "v_mfma_f32_16x16x32_bf16 v[60:63], v[12:15], v[124:127], v[60:63]\n"
    "ds_read_b128 v[12:15], v3 offset:45056\n"
    "s_waitcnt lgkmcnt(6)\n"
    "v_mfma_f32_16x16x32_bf16 v[52:55], v[32:35], v[128:131], v[52:55]\n"
    "v_cvt_pk_bf16_f32 v70, v70, v78\n"
    "v_cvt_pk_bf16_f32 v71, v71, v79\n"
    "v_mfma_f32_16x16x32_bf16 v[60:63], v[32:35], v[132:135], v[60:63]\n"
    "ds_read_b128 v[32:35], v0 offset:45056\n"
    "s_waitcnt lgkmcnt(6)\n"
    "v_mfma_f32_16x16x32_bf16 v[52:55], v[36:39], v[136:139], v[52:55]\n"
    "v_mfma_f32_16x16x32_bf16 v[60:63], v[36:39], v[140:143], v[60:63]\n"
    "ds_read_b128 v[36:39], v3 offset:47104\n"
    "s_waitcnt lgkmcnt(6)\n"
    "v_mfma_f32_16x16x32_bf16 v[52:55], v[20:23], v[144:147], v[52:55]\n"
    "v_mfma_f32_16x16x32_bf16 v[60:63], v[20:23], v[148:151], v[60:63]\n"
    "ds_read_b128 v[20:23], v0 offset:47104\n"
    "s_waitcnt lgkmcnt(6)\n"
    "v_mfma_f32_16x16x32_bf16 v[52:55], v[40:43], v[152:155], v[52:55]\n"
    "buffer_store_dword v68, v16, s[28:31], s80 offen nt\n"
    "v_mfma_f32_16x16x32_bf16 v[60:63], v[40:43], v[156:159], v[60:63]\n"
    "ds_read_b128 v[40:43], v2 offset:32768\n"
    "s_waitcnt lgkmcnt(6)\n"
    "v_mfma_f32_16x16x32_bf16 v[52:55], v[44:47], v[160:163], v[52:55]\n"
    "buffer_store_dword v69, v17, s[28:31], s80 offen nt\n"
    "v_mfma_f32_16x16x32_bf16 v[60:63], v[44:47], v[164:167], v[60:63]\n"
    "ds_read_b128 v[44:47], v7 offset:32768\n"
    "s_waitcnt lgkmcnt(6)\n"
    "v_mfma_f32_16x16x32_bf16 v[52:55], v[8:11], v[168:171], v[52:55]\n"
    "buffer_store_dword v70, v18, s[28:31], s80 offen nt\n"
    "v_mfma_f32_16x16x32_bf16 v[60:63], v[8:11], v[172:175], v[60:63]\n"
    "ds_read_b128 v[8:11], v2 offset:34816\n"
    "s_waitcnt lgkmcnt(6)\n"
    "v_mfma_f32_16x16x32_bf16 v[52:55], v[12:15], v[176:179], v[52:55]\n"
    "buffer_store_dword v71, v19, s[28:31], s80 offen nt\n"
    "v_mfma_f32_16x16x32_bf16 v[60:63], v[12:15], v[180:183], v[60:63]\n"
    "ds_read_b128 v[12:15], v7 offset:34816\n"
    "s_waitcnt lgkmcnt(6)\n"
    "v_mfma_f32_16x16x32_bf16 v[52:55], v[32:35], v[184:187], v[52:55]\n"
    "v_mfma_f32_16x16x32_bf16 v[60:63], v[32:35], v[188:191], v[60:63]\n"
    "ds_read_b128 v[32:35], v2 offset:36864\n"
    "s_waitcnt lgkmcnt(6)\n"
    "v_mfma_f32_16x16x32_bf16 v[52:55], v[36:39], v[192:195], v[52:55]\n"
    "v_mfma_f32_16x16x32_bf16 v[60:63], v[36:39], v[196:199], v[60:63]\n"
    "ds_read_b128 v[36:39], v7 offset:36864\n"
    "s_barrier\n"
    "s_waitcnt lgkmcnt(6)\n"
    "v_mfma_f32_16x16x32_bf16 v[52:55], v[20:23], v[200:203], v[52:55]\n"
    "s_lshl_b32 s77, s60, 3\n"
    "v_mfma_f32_16x16x32_bf16 v[60:63], v[20:23], v[204:207], v[60:63]\n"
    "ds_read_b128 v[20:23], v2 offset:38912\n"
    "s_waitcnt lgkmcnt(6)\n"
    "v_mfma_f32_16x16x32_bf16 v[52:55], v[40:43], v[208:211], v[52:55]\n"
    "v_mfma_f32_16x16x32_bf16 v[60:63], v[40:43], v[212:215], v[60:63]\n"
    "ds_read_b128 v[40:43], v7 offset:38912\n"
    "s_waitcnt lgkmcnt(6)\n"
    "v_mfma_f32_16x16x32_bf16 v[52:55], v[44:47], v[216:219], v[52:55]\n"
    "v_mfma_f32_16x16x32_bf16 v[60:63], v[44:47], v[220:223], v[60:63]\n"
    "ds_read_b128 v[44:47], v2 offset:40960\n"
    "s_waitcnt lgkmcnt(6)\n"
    "v_mfma_f32_16x16x32_bf16 v[52:55], v[8:11], v[224:227], v[52:55]\n"
    "v_mfma_f32_16x16x32_bf16 v[60:63], v[8:11], v[228:231], v[60:63]\n"
    "ds_read_b128 v[8:11], v7 offset:40960\n"
    "s_waitcnt lgkmcnt(6)\n"
    "v_mfma_f32_16x16x32_bf16 v[52:55], v[12:15], v[232:235], v[52:55]\n"
    "v_mfma_f32_16x16x32_bf16 v[60:63], v[12:15], v[236:239], v[60:63]\n"
    "ds_read_b128 v[12:15], v2 offset:43008\n"
    "s_waitcnt lgkmcnt(6)\n"
    "v_mfma_f32_16x16x32_bf16 v[52:55], v[32:35], v[240:243], v[52:55]\n"
    "v_mfma_f32_16x16x32_bf16 v[60:63], v[32:35], v[244:247], v[60:63]\n"
    "ds_read_b128 v[32:35], v7 offset:43008\n"
    "s_waitcnt lgkmcnt(6)\n"
    "v_mfma_f32_16x16x32_bf16 v[52:55], v[36:39], v[248:251], v[52:55]\n"
    "v_mfma_f32_16x16x32_bf16 v[60:63], v[36:39], v[252:255], v[60:63]\n"
    "ds_read_b128 v[36:39], v2 offset:45056\n"
    "s_waitcnt lgkmcnt(6)\n"
    "v_mfma_f32_16x16x32_bf16 v[68:71], v[20:23], v[112:115], 0\n"
    "v_mfma_f32_16x16x32_bf16 v[76:79], v[20:23], v[116:119], 0\n"
    "ds_read_b128 v[20:23], v7 offset:45056\n"
    "s_waitcnt lgkmcnt(6)\n"
    "v_mfma_f32_16x16x32_bf16 v[68:71], v[40:43], v[120:123], v[68:71]\n"
    "s_nop 0\n"
    "v_cvt_pk_bf16_f32 v52, v52, v60\n"
    "v_cvt_pk_bf16_f32 v53, v53, v61\n"
    "v_mfma_f32_16x16x32_bf16 v[76:79], v[40:43], v[124:127], v[76:79]\n"
    "ds_read_b128 v[40:43], v2 offset:47104\n"
    "s_waitcnt lgkmcnt(6)\n"
    "v_mfma_f32_16x16x32_bf16 v[68:71], v[44:47], v[128:131], v[68:71]\n"
    "v_cvt_pk_bf16_f32 v54, v54, v62\n"
    "v_cvt_pk_bf16_f32 v55, v55, v63\n"
    "v_mfma_f32_16x16x32_bf16 v[76:79], v[44:47], v[132:135], v[76:79]\n"
    "ds_read_b128 v[44:47], v7 offset:47104\n"
    "s_waitcnt lgkmcnt(6)\n"
    "v_mfma_f32_16x16x32_bf16 v[68:71], v[8:11], v[136:139], v[68:71]\n"
    "buffer_store_dword v52, v16, s[28:31], s77 offen nt\n"
    "v_mfma_f32_16x16x32_bf16 v[76:79], v[8:11], v[140:143], v[76:79]\n"
    "ds_read_b128 v[8:11], v2 offset:49152\n"
    "s_waitcnt lgkmcnt(6)\n"
    "v_mfma_f32_16x16x32_bf16 v[68:71], v[12:15], v[144:147], v[68:71]\n"
    "buffer_store_dword v53, v17, s[28:31], s77 offen nt\n"
    "v_mfma_f32_16x16x32_bf16 v[76:79], v[12:15], v[148:151], v[76:79]\n"
    "ds_read_b128 v[12:15], v7 offset:49152\n"
    "s_waitcnt lgkmcnt(6)\n"
    "v_mfma_f32_16x16x32_bf16 v[68:71], v[32:35], v[152:155], v[68:71]\n"
    "buffer_store_dword v54, v18, s[28:31], s77 offen nt\n"
    "v_mfma_f32_16x16x32_bf16 v[76:79], v[32:35], v[156:159], v[76:79]\n"
    "ds_read_b128 v[32:35], v2 offset:51200\n"
    "s_waitcnt lgkmcnt(6)\n"
    "v_mfma_f32_16x16x32_bf16 v[68:71], v[36:39], v[160:163], v[68:71]\n"
    "buffer_store_dword v55, v19, s[28:31], s77 offen nt\n"
    "v_mfma_f32_16x16x32_bf16 v[76:79], v[36:39], v[164:167], v[76:79]\n"
    "ds_read_b128 v[36:39], v7 offset:51200\n"
    "s_waitcnt lgkmcnt(6)\n"
    "v_mfma_f32_16x16x32_bf16 v[68:71], v[20:23], v[168:171], v[68:71]\n"
    "v_mfma_f32_16x16x32_bf16 v[76:79], v[20:23], v[172:175], v[76:79]\n"
    "ds_read_b128 v[20:23], v2 offset:53248\n"
    "s_waitcnt lgkmcnt(6)\n"
    "v_mfma_f32_16x16x32_bf16 v[68:71], v[40:43], v[176:179], v[68:71]\n"
    "v_mfma_f32_16x16x32_bf16 v[76:79], v[40:43], v[180:183], v[76:79]\n"
    "ds_read_b128 v[40:43], v7 offset:53248\n"
    "s_waitcnt lgkmcnt(6)\n"
    "v_mfma_f32_16x16x32_bf16 v[68:71], v[44:47], v[184:187], v[68:71]\n"
    "v_mfma_f32_16x16x32_bf16 v[76:79], v[44:47], v[188:191], v[76:79]\n"
    "ds_read_b128 v[44:47], v2 offset:55296\n"
    "s_waitcnt lgkmcnt(6)\n"
    "v_mfma_f32_16x16x32_bf16 v[68:71], v[8:11], v[192:195], v[68:71]\n"
    "v_mfma_f32_16x16x32_bf16 v[76:79], v[8:11], v[196:199], v[76:79]\n"
    "ds_read_b128 v[8:11], v7 offset:55296\n"
    "s_barrier\n"
    "s_waitcnt lgkmcnt(6)\n"
    "v_mfma_f32_16x16x32_bf16 v[68:71], v[12:15], v[200:203], v[68:71]\n"
    "s_mov_b32 s78, 294912\n"
    "s_lshl_b32 s79, s61, 3\n"
    "v_mfma_f32_16x16x32_bf16 v[76:79], v[12:15], v[204:207], v[76:79]\n"
    "ds_read_b128 v[12:15], v2 offset:57344\n"
    "s_waitcnt lgkmcnt(6)\n"
    "v_mfma_f32_16x16x32_bf16 v[68:71], v[32:35], v[208:211], v[68:71]\n"
    "v_mfma_f32_16x16x32_bf16 v[76:79], v[32:35], v[212:215], v[76:79]\n"
    "ds_read_b128 v[32:35], v7 offset:57344\n"
    "s_waitcnt lgkmcnt(6)\n"
    "v_mfma_f32_16x16x32_bf16 v[68:71], v[36:39], v[216:219], v[68:71]\n"
    "v_mfma_f32_16x16x32_bf16 v[76:79], v[36:39], v[220:223], v[76:79]\n"
    "ds_read_b128 v[36:39], v2 offset:59392\n"
    "s_waitcnt lgkmcnt(6)\n"
    "v_mfma_f32_16x16x32_bf16 v[68:71], v[20:23], v[224:227], v[68:71]\n"
    "v_mfma_f32_16x16x32_bf16 v[76:79], v[20:23], v[228:231], v[76:79]\n"
    "ds_read_b128 v[20:23], v7 offset:59392\n"
    "s_waitcnt lgkmcnt(6)\n"
    "v_mfma_f32_16x16x32_bf16 v[68:71], v[40:43], v[232:235], v[68:71]\n"
    "v_mfma_f32_16x16x32_bf16 v[76:79], v[40:43], v[236:239], v[76:79]\n"
    "ds_read_b128 v[40:43], v2 offset:61440\n"
    "s_waitcnt lgkmcnt(6)\n"
    "v_mfma_f32_16x16x32_bf16 v[68:71], v[44:47], v[240:243], v[68:71]\n"
    "v_mfma_f32_16x16x32_bf16 v[76:79], v[44:47], v[244:247], v[76:79]\n"
    "ds_read_b128 v[44:47], v7 offset:61440\n"
    "s_waitcnt lgkmcnt(6)\n"
    "v_mfma_f32_16x16x32_bf16 v[68:71], v[8:11], v[248:251], v[68:71]\n"
    "v_mfma_f32_16x16x32_bf16 v[76:79], v[8:11], v[252:255], v[76:79]\n"
    "ds_read_b128 v[8:11], v2 offset:63488\n"
    "s_waitcnt lgkmcnt(6)\n"
    "v_mfma_f32_16x16x32_bf16 v[52:55], v[12:15], v[112:115], 0\n"
    "v_mfma_f32_16x16x32_bf16 v[60:63], v[12:15], v[116:119], 0\n"
    "ds_read_b128 v[12:15], v7 offset:63488\n"
    "s_waitcnt lgkmcnt(6)\n"
    "v_mfma_f32_16x16x32_bf16 v[52:55], v[32:35], v[120:123], v[52:55]\n"
    "s_nop 0\n"
    "v_cvt_pk_bf16_f32 v68, v68, v76\n"
    "v_cvt_pk_bf16_f32 v69, v69, v77\n"
    "v_mfma_f32_16x16x32_bf16 v[60:63], v[32:35], v[124:127], v[60:63]\n"
    "ds_read_b128 v[32:35], v3 offset:49152\n"
    "s_waitcnt lgkmcnt(6)\n"
    "v_mfma_f32_16x16x32_bf16 v[52:55], v[36:39], v[128:131], v[52:55]\n"
    "v_cvt_pk_bf16_f32 v70, v70, v78\n"
    "v_cvt_pk_bf16_f32 v71, v71, v79\n"
    "v_mfma_f32_16x16x32_bf16 v[60:63], v[36:39], v[132:135], v[60:63]\n"
    "ds_read_b128 v[36:39], v0 offset:49152\n"
    "s_waitcnt lgkmcnt(6)\n"
    "v_mfma_f32_16x16x32_bf16 v[52:55], v[20:23], v[136:139], v[52:55]\n"
    "buffer_store_dword v64, v16, s[28:31], s78 offen nt\n"
    "v_mfma_f32_16x16x32_bf16 v[60:63], v[20:23], v[140:143], v[60:63]\n"
    "ds_read_b128 v[20:23], v3 offset:51200\n"
    "s_waitcnt lgkmcnt(6)\n"
    "v_mfma_f32_16x16x32_bf16 v[52:55], v[40:43], v[144:147], v[52:55]\n"
    "buffer_store_dword v65, v17, s[28:31], s78 offen nt\n"
    "v_mfma_f32_16x16x32_bf16 v[60:63], v[40:43], v[148:151], v[60:63]\n"
    "ds_read_b128 v[40:43], v0 offset:51200\n"
    "s_waitcnt lgkmcnt(6)\n"
    "v_mfma_f32_16x16x32_bf16 v[52:55], v[44:47], v[152:155], v[52:55]\n"
    "buffer_store_dword v66, v18, s[28:31], s78 offen nt\n"
    "v_mfma_f32_16x16x32_bf16 v[60:63], v[44:47], v[156:159], v[60:63]\n"
    "ds_read_b128 v[44:47], v3 offset:53248\n"
    "s_waitcnt lgkmcnt(6)\n"
    "v_mfma_f32_16x16x32_bf16 v[52:55], v[8:11], v[160:163], v[52:55]\n"
    "buffer_store_dword v67, v19, s[28:31], s78 offen nt\n"
    "v_mfma_f32_16x16x32_bf16 v[60:63], v[8:11], v[164:167], v[60:63]\n"
    "ds_read_b128 v[8:11], v0 offset:53248\n"
    "s_waitcnt lgkmcnt(6)\n"
    "v_mfma_f32_16x16x32_bf16 v[52:55], v[12:15], v[168:171], v[52:55]\n"
    "buffer_store_dword v68, v16, s[28:31], s79 offen nt\n"
    "v_mfma_f32_16x16x32_bf16 v[60:63], v[12:15], v[172:175], v[60:63]\n"
    "ds_read_b128 v[12:15], v3 offset:55296\n"
    "s_waitcnt lgkmcnt(6)\n"
    "v_mfma_f32_16x16x32_bf16 v[52:55], v[32:35], v[176:179], v[52:55]\n"
    "buffer_store_dword v69, v17, s[28:31], s79 offen nt\n"
    "v_mfma_f32_16x16x32_bf16 v[60:63], v[32:35], v[180:183], v[60:63]\n"
    "ds_read_b128 v[32:35], v0 offset:55296\n"
    "s_waitcnt lgkmcnt(6)\n"
    "v_mfma_f32_16x16x32_bf16 v[52:55], v[36:39], v[184:187], v[52:55]\n"
    "buffer_store_dword v70, v18, s[28:31], s79 offen nt\n"
    "v_mfma_f32_16x16x32_bf16 v[60:63], v[36:39], v[188:191], v[60:63]\n"
    "ds_read_b128 v[36:39], v3 offset:57344\n"
    "s_waitcnt lgkmcnt(6)\n"
    "v_mfma_f32_16x16x32_bf16 v[52:55], v[20:23], v[192:195], v[52:55]\n"
    "buffer_store_dword v71, v19, s[28:31], s79 offen nt\n"
    "v_mfma_f32_16x16x32_bf16 v[60:63], v[20:23], v[196:199], v[60:63]\n"
    "ds_read_b128 v[20:23], v0 offset:57344\n"
    "s_barrier\n"
    "s_waitcnt lgkmcnt(6)\n"
    "v_mfma_f32_16x16x32_bf16 v[52:55], v[40:43], v[200:203], v[52:55]\n"
    "s_mov_b32 s80, 442368\n"
    "s_lshl_b32 s77, s62, 3\n"
    "v_mfma_f32_16x16x32_bf16 v[60:63], v[40:43], v[204:207], v[60:63]\n"
    "ds_read_b128 v[40:43], v3 offset:59392\n"
    "s_waitcnt lgkmcnt(6)\n"
    "v_mfma_f32_16x16x32_bf16 v[52:55], v[44:47], v[208:211], v[52:55]\n"
    "v_mfma_f32_16x16x32_bf16 v[60:63], v[44:47], v[212:215], v[60:63]\n"
    "ds_read_b128 v[44:47], v0 offset:59392\n"
    "s_waitcnt lgkmcnt(6)\n"
    "v_mfma_f32_16x16x32_bf16 v[52:55], v[8:11], v[216:219], v[52:55]\n"
    "v_mfma_f32_16x16x32_bf16 v[60:63], v[8:11], v[220:223], v[60:63]\n"
    "ds_read_b128 v[8:11], v3 offset:61440\n"
    "s_waitcnt lgkmcnt(6)\n"
    "v_mfma_f32_16x16x32_bf16 v[52:55], v[12:15], v[224:227], v[52:55]\n"
    "v_mfma_f32_16x16x32_bf16 v[60:63], v[12:15], v[228:231], v[60:63]\n"
    "ds_read_b128 v[12:15], v0 offset:61440\n"
    "s_waitcnt lgkmcnt(6)\n"
    "v_mfma_f32_16x16x32_bf16 v[52:55], v[32:35], v[232:235], v[52:55]\n"
    "v_mfma_f32_16x16x32_bf16 v[60:63], v[32:35], v[236:239], v[60:63]\n"
    "ds_read_b128 v[32:35], v3 offset:63488\n"
    "s_waitcnt lgkmcnt(6)\n"
    "v_mfma_f32_16x16x32_bf16 v[52:55], v[36:39], v[240:243], v[52:55]\n"
    "v_mfma_f32_16x16x32_bf16 v[60:63], v[36:39], v[244:247], v[60:63]\n"
    "ds_read_b128 v[36:39], v0 offset:63488\n"
    "s_waitcnt lgkmcnt(6)\n"
    "v_mfma_f32_16x16x32_bf16 v[52:55], v[20:23], v[248:251], v[52:55]\n"
    "v_mfma_f32_16x16x32_bf16 v[60:63], v[20:23], v[252:255], v[60:63]\n"
    "ds_read_b128 v[20:23], v2 offset:0\n"
    "s_waitcnt lgkmcnt(6)\n"
    "v_mfma_f32_16x16x32_bf16 v[68:71], v[40:43], v[112:115], 0\n"
    "v_mfma_f32_16x16x32_bf16 v[76:79], v[40:43], v[116:119], 0\n"
    "ds_read_b128 v[40:43], v7 offset:0\n"
    "s_waitcnt lgkmcnt(6)\n"
    "v_mfma_f32_16x16x32_bf16 v[68:71], v[44:47], v[120:123], v[68:71]\n"
    "s_nop 0\n"
    "v_cvt_pk_bf16_f32 v52, v52, v60\n"
    "v_cvt_pk_bf16_f32 v53, v53, v61\n"
    "v_mfma_f32_16x16x32_bf16 v[76:79], v[44:47], v[124:127], v[76:79]\n"
    "ds_read_b128 v[44:47], v2 offset:2048\n"
    "s_waitcnt lgkmcnt(6)\n"
    "v_mfma_f32_16x16x32_bf16 v[68:71], v[8:11], v[128:131], v[68:71]\n"
    "v_cvt_pk_bf16_f32 v54, v54, v62\n"
    "v_cvt_pk_bf16_f32 v55, v55, v63\n"
    "v_mfma_f32_16x16x32_bf16 v[76:79], v[8:11], v[132:135], v[76:79]\n"
    "ds_read_b128 v[8:11], v7 offset:2048\n"
    "s_waitcnt lgkmcnt(6)\n"
    "v_mfma_f32_16x16x32_bf16 v[68:71], v[12:15], v[136:139], v[68:71]\n"
    "buffer_store_dword v72, v16, s[28:31], s80 offen nt\n"
    "v_mfma_f32_16x16x32_bf16 v[76:79], v[12:15], v[140:143], v[76:79]\n"
    "ds_read_b128 v[12:15], v2 offset:4096\n"
    "s_waitcnt lgkmcnt(6)\n"
    "v_mfma_f32_16x16x32_bf16 v[68:71], v[32:35], v[144:147], v[68:71]\n"
    "buffer_store_dword v73, v17, s[28:31], s80 offen nt\n"
    "v_mfma_f32_16x16x32_bf16 v[76:79], v[32:35], v[148:151], v[76:79]\n"
    "ds_read_b128 v[32:35], v7 offset:4096\n"
    "s_waitcnt lgkmcnt(6)\n"
    "v_mfma_f32_16x16x32_bf16 v[68:71], v[36:39], v[152:155], v[68:71]\n"
    "buffer_store_dword v74, v18, s[28:31], s80 offen nt\n"
    "v_mfma_f32_16x16x32_bf16 v[76:79], v[36:39], v[156:159], v[76:79]\n"
    "ds_read_b128 v[36:39], v2 offset:6144\n"
    "s_waitcnt lgkmcnt(6)\n"
    "v_mfma_f32_16x16x32_bf16 v[68:71], v[20:23], v[160:163], v[68:71]\n"
    "buffer_store_dword v75, v19, s[28:31], s80 offen nt\n"
    "v_mfma_f32_16x16x32_bf16 v[76:79], v[20:23], v[164:167], v[76:79]\n"
    "ds_read_b128 v[20:23], v7 offset:6144\n"
    "s_waitcnt lgkmcnt(6)\n"
    "v_mfma_f32_16x16x32_bf16 v[68:71], v[40:43], v[168:171], v[68:71]\n"
    "buffer_store_dword v52, v16, s[28:31], s77 offen nt\n"
    "v_mfma_f32_16x16x32_bf16 v[76:79], v[40:43], v[172:175], v[76:79]\n"
    "ds_read_b128 v[40:43], v2 offset:8192\n"
    "s_waitcnt lgkmcnt(6)\n"
    "v_mfma_f32_16x16x32_bf16 v[68:71], v[44:47], v[176:179], v[68:71]\n"
    "buffer_store_dword v53, v17, s[28:31], s77 offen nt\n"
    "v_mfma_f32_16x16x32_bf16 v[76:79], v[44:47], v[180:183], v[76:79]\n"
    "ds_read_b128 v[44:47], v7 offset:8192\n"
    "s_waitcnt lgkmcnt(6)\n"
    "v_mfma_f32_16x16x32_bf16 v[68:71], v[8:11], v[184:187], v[68:71]\n"
    "buffer_store_dword v54, v18, s[28:31], s77 offen nt\n"
    "v_mfma_f32_16x16x32_bf16 v[76:79], v[8:11], v[188:191], v[76:79]\n"
    "ds_read_b128 v[8:11], v2 offset:10240\n"
    "s_waitcnt lgkmcnt(6)\n"
    "v_mfma_f32_16x16x32_bf16 v[68:71], v[12:15], v[192:195], v[68:71]\n"
    "buffer_store_dword v55, v19, s[28:31], s77 offen nt\n"
    "v_mfma_f32_16x16x32_bf16 v[76:79], v[12:15], v[196:199], v[76:79]\n"
    "ds_read_b128 v[12:15], v7 offset:10240\n"
    "s_barrier\n"
    "s_waitcnt lgkmcnt(6)\n"
    "v_mfma_f32_16x16x32_bf16 v[68:71], v[32:35], v[200:203], v[68:71]\n"
    "s_mov_b32 s78, 589824\n"
    "s_lshl_b32 s79, s63, 3\n"
    "v_mfma_f32_16x16x32_bf16 v[76:79], v[32:35], v[204:207], v[76:79]\n"
    "ds_read_b128 v[32:35], v2 offset:12288\n"
    "s_waitcnt lgkmcnt(6)\n"
    "v_mfma_f32_16x16x32_bf16 v[68:71], v[36:39], v[208:211], v[68:71]\n"
    "v_mfma_f32_16x16x32_bf16 v[76:79], v[36:39], v[212:215], v[76:79]\n"
    "ds_read_b128 v[36:39], v7 offset:12288\n"
    "s_waitcnt lgkmcnt(6)\n"
    "v_mfma_f32_16x16x32_bf16 v[68:71], v[20:23], v[216:219], v[68:71]\n"
    "v_mfma_f32_16x16x32_bf16 v[76:79], v[20:23], v[220:223], v[76:79]\n"
    "ds_read_b128 v[20:23], v2 offset:14336\n"
    "s_waitcnt lgkmcnt(6)\n"
    "v_mfma_f32_16x16x32_bf16 v[68:71], v[40:43], v[224:227], v[68:71]\n"
    "v_mfma_f32_16x16x32_bf16 v[76:79], v[40:43], v[228:231], v[76:79]\n"
    "ds_read_b128 v[40:43], v7 offset:14336\n"
    "s_waitcnt lgkmcnt(6)\n"
    "v_mfma_f32_16x16x32_bf16 v[68:71], v[44:47], v[232:235], v[68:71]\n"
    "v_mfma_f32_16x16x32_bf16 v[76:79], v[44:47], v[236:239], v[76:79]\n"
    "ds_read_b128 v[44:47], v2 offset:16384\n"
    "s_waitcnt lgkmcnt(6)\n"
    "v_mfma_f32_16x16x32_bf16 v[68:71], v[8:11], v[240:243], v[68:71]\n"
    "v_mfma_f32_16x16x32_bf16 v[76:79], v[8:11], v[244:247], v[76:79]\n"
    "ds_read_b128 v[8:11], v7 offset:16384\n"
    "s_waitcnt lgkmcnt(6)\n"
    "v_mfma_f32_16x16x32_bf16 v[68:71], v[12:15], v[248:251], v[68:71]\n"
    "v_mfma_f32_16x16x32_bf16 v[76:79], v[12:15], v[252:255], v[76:79]\n"
    "ds_read_b128 v[12:15], v2 offset:18432\n"
    "s_waitcnt lgkmcnt(6)\n"
    "v_mfma_f32_16x16x32_bf16 v[52:55], v[32:35], v[112:115], 0\n"
    "v_mfma_f32_16x16x32_bf16 v[60:63], v[32:35], v[116:119], 0\n"
    "ds_read_b128 v[32:35], v7 offset:18432\n"
    "s_waitcnt lgkmcnt(6)\n"
    "v_mfma_f32_16x16x32_bf16 v[52:55], v[36:39], v[120:123], v[52:55]\n"
    "s_nop 0\n"
    "v_cvt_pk_bf16_f32 v68, v68, v76\n"
    "v_cvt_pk_bf16_f32 v69, v69, v77\n"
    "v_mfma_f32_16x16x32_bf16 v[60:63], v[36:39], v[124:127], v[60:63]\n"
    "ds_read_b128 v[36:39], v2 offset:20480\n"
    "s_waitcnt lgkmcnt(6)\n"
    "v_mfma_f32_16x16x32_bf16 v[52:55], v[20:23], v[128:131], v[52:55]\n"
    "v_cvt_pk_bf16_f32 v70, v70, v78\n"
    "v_cvt_pk_bf16_f32 v71, v71, v79\n"
    "v_mfma_f32_16x16x32_bf16 v[60:63], v[20:23], v[132:135], v[60:63]\n"
    "ds_read_b128 v[20:23], v7 offset:20480\n"
    "s_waitcnt lgkmcnt(6)\n"
    "v_mfma_f32_16x16x32_bf16 v[52:55], v[40:43], v[136:139], v[52:55]\n"
    "buffer_store_dword v80, v16, s[28:31], s78 offen nt\n"
    "v_mfma_f32_16x16x32_bf16 v[60:63], v[40:43], v[140:143], v[60:63]\n"
    "ds_read_b128 v[40:43], v2 offset:22528\n"
    "s_waitcnt lgkmcnt(6)\n"
    "v_mfma_f32_16x16x32_bf16 v[52:55], v[44:47], v[144:147], v[52:55]\n"
    "buffer_store_dword v81, v17, s[28:31], s78 offen nt\n"
    "v_mfma_f32_16x16x32_bf16 v[60:63], v[44:47], v[148:151], v[60:63]\n"
    "ds_read_b128 v[44:47], v7 offset:22528\n"
    "s_waitcnt lgkmcnt(6)\n"
    "v_mfma_f32_16x16x32_bf16 v[52:55], v[8:11], v[152:155], v[52:55]\n"
    "buffer_store_dword v82, v18, s[28:31], s78 offen nt\n"
    "v_mfma_f32_16x16x32_bf16 v[60:63], v[8:11], v[156:159], v[60:63]\n"
    "ds_read_b128 v[8:11], v2 offset:24576\n"
    "s_waitcnt lgkmcnt(6)\n"
    "v_mfma_f32_16x16x32_bf16 v[52:55], v[12:15], v[160:163], v[52:55]\n"
    "buffer_store_dword v83, v19, s[28:31], s78 offen nt\n"
    "v_mfma_f32_16x16x32_bf16 v[60:63], v[12:15], v[164:167], v[60:63]\n"
    "ds_read_b128 v[12:15], v7 offset:24576\n"
    "s_waitcnt lgkmcnt(6)\n"
    "v_mfma_f32_16x16x32_bf16 v[52:55], v[32:35], v[168:171], v[52:55]\n"
    "buffer_store_dword v68, v16, s[28:31], s79 offen nt\n"
    "v_mfma_f32_16x16x32_bf16 v[60:63], v[32:35], v[172:175], v[60:63]\n"
    "ds_read_b128 v[32:35], v2 offset:26624\n"
    "s_waitcnt lgkmcnt(6)\n"
    "v_mfma_f32_16x16x32_bf16 v[52:55], v[36:39], v[176:179], v[52:55]\n"
    "buffer_store_dword v69, v17, s[28:31], s79 offen nt\n"
    "v_mfma_f32_16x16x32_bf16 v[60:63], v[36:39], v[180:183], v[60:63]\n"
    "ds_read_b128 v[36:39], v7 offset:26624\n"
    "s_waitcnt lgkmcnt(6)\n"
    "v_mfma_f32_16x16x32_bf16 v[52:55], v[20:23], v[184:187], v[52:55]\n"
    "buffer_store_dword v70, v18, s[28:31], s79 offen nt\n"
    "v_mfma_f32_16x16x32_bf16 v[60:63], v[20:23], v[188:191], v[60:63]\n"
    "ds_read_b128 v[20:23], v2 offset:28672\n"
    "s_waitcnt lgkmcnt(6)\n"
    "v_mfma_f32_16x16x32_bf16 v[52:55], v[40:43], v[192:195], v[52:55]\n"
    "buffer_store_dword v71, v19, s[28:31], s79 offen nt\n"
    "v_mfma_f32_16x16x32_bf16 v[60:63], v[40:43], v[196:199], v[60:63]\n"
    "ds_read_b128 v[40:43], v7 offset:28672\n"
    "s_barrier\n"
    "s_waitcnt lgkmcnt(6)\n"
    "v_mfma_f32_16x16x32_bf16 v[52:55], v[44:47], v[200:203], v[52:55]\n"
    "s_mov_b32 s80, 737280\n"
    "s_lshl_b32 s77, s64, 3\n"
    "v_mfma_f32_16x16x32_bf16 v[60:63], v[44:47], v[204:207], v[60:63]\n"
    "ds_read_b128 v[44:47], v2 offset:30720\n"
    "s_waitcnt lgkmcnt(6)\n"
    "v_mfma_f32_16x16x32_bf16 v[52:55], v[8:11], v[208:211], v[52:55]\n"
    "v_mfma_f32_16x16x32_bf16 v[60:63], v[8:11], v[212:215], v[60:63]\n"
    "ds_read_b128 v[8:11], v7 offset:30720\n"
    "s_waitcnt lgkmcnt(6)\n"
    "v_mfma_f32_16x16x32_bf16 v[52:55], v[12:15], v[216:219], v[52:55]\n"
    "v_mfma_f32_16x16x32_bf16 v[60:63], v[12:15], v[220:223], v[60:63]\n"
    "ds_read_b128 v[12:15], v6 offset:0\n"
    "s_waitcnt lgkmcnt(6)\n"
    "v_mfma_f32_16x16x32_bf16 v[52:55], v[32:35], v[224:227], v[52:55]\n"
    "v_mfma_f32_16x16x32_bf16 v[60:63], v[32:35], v[228:231], v[60:63]\n"
    "ds_read_b128 v[32:35], v4 offset:0\n"
    "s_waitcnt lgkmcnt(6)\n"
    "v_mfma_f32_16x16x32_bf16 v[52:55], v[36:39], v[232:235], v[52:55]\n"
    "v_mfma_f32_16x16x32_bf16 v[60:63], v[36:39], v[236:239], v[60:63]\n"
    "ds_read_b128 v[36:39], v6 offset:2048\n"
    "s_waitcnt lgkmcnt(6)\n"
    "v_mfma_f32_16x16x32_bf16 v[52:55], v[20:23], v[240:243], v[52:55]\n"
    "v_mfma_f32_16x16x32_bf16 v[60:63], v[20:23], v[244:247], v[60:63]\n"
    "ds_read_b128 v[20:23], v4 offset:2048\n"
    "s_waitcnt lgkmcnt(6)\n"
    "v_mfma_f32_16x16x32_bf16 v[52:55], v[40:43], v[248:251], v[52:55]\n"
    "v_mfma_f32_16x16x32_bf16 v[60:63], v[40:43], v[252:255], v[60:63]\n"
    "ds_read_b128 v[40:43], v6 offset:4096\n"
    "s_waitcnt lgkmcnt(6)\n"
    "v_mfma_f32_16x16x32_bf16 v[68:71], v[44:47], v[112:115], 0\n"
    "v_mfma_f32_16x16x32_bf16 v[76:79], v[44:47], v[116:119], 0\n"
    "ds_read_b128 v[44:47], v4 offset:4096\n"
    "s_waitcnt lgkmcnt(6)\n"
    "v_mfma_f32_16x16x32_bf16 v[68:71], v[8:11], v[120:123], v[68:71]\n"
    "s_nop 0\n"
    "v_cvt_pk_bf16_f32 v52, v52, v60\n"
    "v_cvt_pk_bf16_f32 v53, v53, v61\n"
    "v_mfma_f32_16x16x32_bf16 v[76:79], v[8:11], v[124:127], v[76:79]\n"
    "ds_read_b128 v[8:11], v6 offset:6144\n"
    "s_waitcnt lgkmcnt(6)\n"
    "v_mfma_f32_16x16x32_bf16 v[68:71], v[12:15], v[128:131], v[68:71]\n"
    "v_cvt_pk_bf16_f32 v54, v54, v62\n"
    "v_cvt_pk_bf16_f32 v55, v55, v63\n"
    "v_mfma_f32_16x16x32_bf16 v[76:79], v[12:15], v[132:135], v[76:79]\n"
    "ds_read_b128 v[12:15], v4 offset:6144\n"
    "s_waitcnt lgkmcnt(6)\n"
    "v_mfma_f32_16x16x32_bf16 v[68:71], v[32:35], v[136:139], v[68:71]\n"
    "buffer_store_dword v88, v16, s[28:31], s80 offen nt\n"
    "v_mfma_f32_16x16x32_bf16 v[76:79], v[32:35], v[140:143], v[76:79]\n"
    "ds_read_b128 v[32:35], v6 offset:8192\n"
    "s_waitcnt lgkmcnt(6)\n"
    "v_mfma_f32_16x16x32_bf16 v[68:71], v[36:39], v[144:147], v[68:71]\n"
    "buffer_store_dword v89, v17, s[28:31], s80 offen nt\n"
    "v_mfma_f32_16x16x32_bf16 v[76:79], v[36:39], v[148:151], v[76:79]\n"
    "ds_read_b128 v[36:39], v4 offset:8192\n"
    "s_waitcnt lgkmcnt(6)\n"
    "v_mfma_f32_16x16x32_bf16 v[68:71], v[20:23], v[152:155], v[68:71]\n"
    "buffer_store_dword v90, v18, s[28:31], s80 offen nt\n"
    "v_mfma_f32_16x16x32_bf16 v[76:79], v[20:23], v[156:159], v[76:79]\n"
    "ds_read_b128 v[20:23], v6 offset:10240\n"
    "s_waitcnt lgkmcnt(6)\n"
    "v_mfma_f32_16x16x32_bf16 v[68:71], v[40:43], v[160:163], v[68:71]\n"
    "buffer_store_dword v91, v19, s[28:31], s80 offen nt\n"
    "v_mfma_f32_16x16x32_bf16 v[76:79], v[40:43], v[164:167], v[76:79]\n"
    "ds_read_b128 v[40:43], v4 offset:10240\n"
    "s_waitcnt lgkmcnt(6)\n"
    "v_mfma_f32_16x16x32_bf16 v[68:71], v[44:47], v[168:171], v[68:71]\n"
    "buffer_store_dword v52, v16, s[28:31], s77 offen nt\n"
    "v_mfma_f32_16x16x32_bf16 v[76:79], v[44:47], v[172:175], v[76:79]\n"
    "ds_read_b128 v[44:47], v6 offset:12288\n"
    "s_waitcnt lgkmcnt(6)\n"
    "v_mfma_f32_16x16x32_bf16 v[68:71], v[8:11], v[176:179], v[68:71]\n"
    "buffer_store_dword v53, v17, s[28:31], s77 offen nt\n"
    "v_mfma_f32_16x16x32_bf16 v[76:79], v[8:11], v[180:183], v[76:79]\n"
    "ds_read_b128 v[8:11], v4 offset:12288\n"
    "s_waitcnt lgkmcnt(6)\n"
    "v_mfma_f32_16x16x32_bf16 v[68:71], v[12:15], v[184:187], v[68:71]\n"
    "buffer_store_dword v54, v18, s[28:31], s77 offen nt\n"
    "v_mfma_f32_16x16x32_bf16 v[76:79], v[12:15], v[188:191], v[76:79]\n"
    "ds_read_b128 v[12:15], v6 offset:14336\n"
    "s_waitcnt lgkmcnt(6)\n"
    "v_mfma_f32_16x16x32_bf16 v[68:71], v[32:35], v[192:195], v[68:71]\n"
    "buffer_store_dword v55, v19, s[28:31], s77 offen nt\n"
    "v_mfma_f32_16x16x32_bf16 v[76:79], v[32:35], v[196:199], v[76:79]\n"
    "ds_read_b128 v[32:35], v4 offset:14336\n"
    "s_barrier\n"
    "s_waitcnt lgkmcnt(6)\n"
    "v_mfma_f32_16x16x32_bf16 v[68:71], v[36:39], v[200:203], v[68:71]\n"
    "s_mov_b32 s78, 884736\n"
    "s_lshl_b32 s79, s65, 3\n"
    "v_mfma_f32_16x16x32_bf16 v[76:79], v[36:39], v[204:207], v[76:79]\n"
    "ds_read_b128 v[36:39], v6 offset:16384\n"
    "s_waitcnt lgkmcnt(6)\n"
    "v_mfma_f32_16x16x32_bf16 v[68:71], v[20:23], v[208:211], v[68:71]\n"
    "v_mfma_f32_16x16x32_bf16 v[76:79], v[20:23], v[212:215], v[76:79]\n"
    "ds_read_b128 v[20:23], v4 offset:16384\n"
    "s_waitcnt lgkmcnt(6)\n"
    "v_mfma_f32_16x16x32_bf16 v[68:71], v[40:43], v[216:219], v[68:71]\n"
    "v_mfma_f32_16x16x32_bf16 v[76:79], v[40:43], v[220:223], v[76:79]\n"
    "ds_read_b128 v[40:43], v6 offset:18432\n"
    "s_waitcnt lgkmcnt(6)\n"
    "v_mfma_f32_16x16x32_bf16 v[68:71], v[44:47], v[224:227], v[68:71]\n"
    "v_mfma_f32_16x16x32_bf16 v[76:79], v[44:47], v[228:231], v[76:79]\n"
    "ds_read_b128 v[44:47], v4 offset:18432\n"
    "s_waitcnt lgkmcnt(6)\n"
    "v_mfma_f32_16x16x32_bf16 v[68:71], v[8:11], v[232:235], v[68:71]\n"
    "v_mfma_f32_16x16x32_bf16 v[76:79], v[8:11], v[236:239], v[76:79]\n"
    "ds_read_b128 v[8:11], v6 offset:20480\n"
    "s_waitcnt lgkmcnt(6)\n"
    "v_mfma_f32_16x16x32_bf16 v[68:71], v[12:15], v[240:243], v[68:71]\n"
    "v_mfma_f32_16x16x32_bf16 v[76:79], v[12:15], v[244:247], v[76:79]\n"
    "ds_read_b128 v[12:15], v4 offset:20480\n"
    "s_waitcnt lgkmcnt(6)\n"
    "v_mfma_f32_16x16x32_bf16 v[68:71], v[32:35], v[248:251], v[68:71]\n"
    "v_mfma_f32_16x16x32_bf16 v[76:79], v[32:35], v[252:255], v[76:79]\n"
    "ds_read_b128 v[32:35], v6 offset:22528\n"
    "s_waitcnt lgkmcnt(6)\n"
    "v_mfma_f32_16x16x32_bf16 v[52:55], v[36:39], v[112:115], 0\n"
    "v_mfma_f32_16x16x32_bf16 v[60:63], v[36:39], v[116:119], 0\n"
    "ds_read_b128 v[36:39], v4 offset:22528\n"
    "s_waitcnt lgkmcnt(6)\n"
    "v_mfma_f32_16x16x32_bf16 v[52:55], v[20:23], v[120:123], v[52:55]\n"
    "s_nop 0\n"
    "v_cvt_pk_bf16_f32 v68, v68, v76\n"
    "v_cvt_pk_bf16_f32 v69, v69, v77\n"
    "v_mfma_f32_16x16x32_bf16 v[60:63], v[20:23], v[124:127], v[60:63]\n"
    "ds_read_b128 v[20:23], v6 offset:24576\n"
    "s_waitcnt lgkmcnt(6)\n"
    "v_mfma_f32_16x16x32_bf16 v[52:55], v[40:43], v[128:131], v[52:55]\n"
    "v_cvt_pk_bf16_f32 v70, v70, v78\n"
    "v_cvt_pk_bf16_f32 v71, v71, v79\n"
    "v_mfma_f32_16x16x32_bf16 v[60:63], v[40:43], v[132:135], v[60:63]\n"
    "ds_read_b128 v[40:43], v4 offset:24576\n"
    "s_waitcnt lgkmcnt(6)\n"
    "v_mfma_f32_16x16x32_bf16 v[52:55], v[44:47], v[136:139], v[52:55]\n"
    "buffer_store_dword v96, v16, s[28:31], s78 offen nt\n"
    "v_mfma_f32_16x16x32_bf16 v[60:63], v[44:47], v[140:143], v[60:63]\n"
    "ds_read_b128 v[44:47], v6 offset:26624\n"
    "s_waitcnt lgkmcnt(6)\n"
    "v_mfma_f32_16x16x32_bf16 v[52:55], v[8:11], v[144:147], v[52:55]\n"
    "buffer_store_dword v97, v17, s[28:31], s78 offen nt\n"
    "v_mfma_f32_16x16x32_bf16 v[60:63], v[8:11], v[148:151], v[60:63]\n"
    "ds_read_b128 v[8:11], v4 offset:26624\n"
    "s_waitcnt lgkmcnt(6)\n"
    "v_mfma_f32_16x16x32_bf16 v[52:55], v[12:15], v[152:155], v[52:55]\n"
    "buffer_store_dword v98, v18, s[28:31], s78 offen nt\n"
    "v_mfma_f32_16x16x32_bf16 v[60:63], v[12:15], v[156:159], v[60:63]\n"
    "ds_read_b128 v[12:15], v6 offset:28672\n"
    "s_waitcnt lgkmcnt(6)\n"
    "v_mfma_f32_16x16x32_bf16 v[52:55], v[32:35], v[160:163], v[52:55]\n"
    "buffer_store_dword v99, v19, s[28:31], s78 offen nt\n"
    "v_mfma_f32_16x16x32_bf16 v[60:63], v[32:35], v[164:167], v[60:63]\n"
    "ds_read_b128 v[32:35], v4 offset:28672\n"
    "s_waitcnt lgkmcnt(6)\n"
    "v_mfma_f32_16x16x32_bf16 v[52:55], v[36:39], v[168:171], v[52:55]\n"
    "buffer_store_dword v68, v16, s[28:31], s79 offen nt\n"
    "v_mfma_f32_16x16x32_bf16 v[60:63], v[36:39], v[172:175], v[60:63]\n"
    "ds_read_b128 v[36:39], v6 offset:30720\n"
    "s_waitcnt lgkmcnt(6)\n"
    "v_mfma_f32_16x16x32_bf16 v[52:55], v[20:23], v[176:179], v[52:55]\n"
    "buffer_store_dword v69, v17, s[28:31], s79 offen nt\n"
    "v_mfma_f32_16x16x32_bf16 v[60:63], v[20:23], v[180:183], v[60:63]\n"
    "ds_read_b128 v[20:23], v4 offset:30720\n"
    "s_waitcnt lgkmcnt(6)\n"
    "v_mfma_f32_16x16x32_bf16 v[52:55], v[40:43], v[184:187], v[52:55]\n"
    "buffer_store_dword v70, v18, s[28:31], s79 offen nt\n"
    "v_mfma_f32_16x16x32_bf16 v[60:63], v[40:43], v[188:191], v[60:63]\n"
    "ds_read_b128 v[40:43], v3 offset:0\n"
    "s_waitcnt lgkmcnt(6)\n"
    "v_mfma_f32_16x16x32_bf16 v[52:55], v[44:47], v[192:195], v[52:55]\n"
    "buffer_store_dword v71, v19, s[28:31], s79 offen nt\n"
    "v_mfma_f32_16x16x32_bf16 v[60:63], v[44:47], v[196:199], v[60:63]\n"
    "ds_read_b128 v[44:47], v0 offset:0\n"
    "s_barrier\n"
    "s_waitcnt lgkmcnt(6)\n"
    "v_mfma_f32_16x16x32_bf16 v[52:55], v[8:11], v[200:203], v[52:55]\n"
    "s_mov_b32 s80, 1032192\n"
    "s_lshl_b32 s77, s66, 3\n"
    "v_mfma_f32_16x16x32_bf16 v[60:63], v[8:11], v[204:207], v[60:63]\n"
    "ds_read_b128 v[8:11], v3 offset:2048\n"
    "s_waitcnt lgkmcnt(6)\n"
    "v_mfma_f32_16x16x32_bf16 v[52:55], v[12:15], v[208:211], v[52:55]\n"
    "v_mfma_f32_16x16x32_bf16 v[60:63], v[12:15], v[212:215], v[60:63]\n"
    "ds_read_b128 v[12:15], v0 offset:2048\n"
    "s_waitcnt lgkmcnt(6)\n"
    "v_mfma_f32_16x16x32_bf16 v[52:55], v[32:35], v[216:219], v[52:55]\n"
    "v_mfma_f32_16x16x32_bf16 v[60:63], v[32:35], v[220:223], v[60:63]\n"
    "ds_read_b128 v[32:35], v3 offset:4096\n"
    "s_waitcnt lgkmcnt(6)\n"
    "v_mfma_f32_16x16x32_bf16 v[52:55], v[36:39], v[224:227], v[52:55]\n"
    "v_mfma_f32_16x16x32_bf16 v[60:63], v[36:39], v[228:231], v[60:63]\n"
    "ds_read_b128 v[36:39], v0 offset:4096\n"
    "s_waitcnt lgkmcnt(6)\n"
    "v_mfma_f32_16x16x32_bf16 v[52:55], v[20:23], v[232:235], v[52:55]\n"
    "v_mfma_f32_16x16x32_bf16 v[60:63], v[20:23], v[236:239], v[60:63]\n"
    "ds_read_b128 v[20:23], v3 offset:6144\n"
    "s_waitcnt lgkmcnt(6)\n"
    "v_mfma_f32_16x16x32_bf16 v[52:55], v[40:43], v[240:243], v[52:55]\n"
    "v_mfma_f32_16x16x32_bf16 v[60:63], v[40:43], v[244:247], v[60:63]\n"
    "ds_read_b128 v[40:43], v0 offset:6144\n"
    "s_waitcnt lgkmcnt(6)\n"
    "v_mfma_f32_16x16x32_bf16 v[52:55], v[44:47], v[248:251], v[52:55]\n"
    "v_mfma_f32_16x16x32_bf16 v[60:63], v[44:47], v[252:255], v[60:63]\n"
    "ds_read_b128 v[44:47], v3 offset:8192\n"
    "s_waitcnt lgkmcnt(6)\n"
    "v_mfma_f32_16x16x32_bf16 v[68:71], v[8:11], v[112:115], 0\n"
    "v_mfma_f32_16x16x32_bf16 v[76:79], v[8:11], v[116:119], 0\n"
    "ds_read_b128 v[8:11], v0 offset:8192\n"
    "s_waitcnt lgkmcnt(6)\n"
    "v_mfma_f32_16x16x32_bf16 v[68:71], v[12:15], v[120:123], v[68:71]\n"
    "s_nop 0\n"
    "v_cvt_pk_bf16_f32 v52, v52, v60\n"
    "v_cvt_pk_bf16_f32 v53, v53, v61\n"
    "v_mfma_f32_16x16x32_bf16 v[76:79], v[12:15], v[124:127], v[76:79]\n"
    "ds_read_b128 v[12:15], v3 offset:10240\n"
    "s_waitcnt lgkmcnt(6)\n"
    "v_mfma_f32_16x16x32_bf16 v[68:71], v[32:35], v[128:131], v[68:71]\n"
    "v_cvt_pk_bf16_f32 v54, v54, v62\n"
    "v_cvt_pk_bf16_f32 v55, v55, v63\n"
    "v_mfma_f32_16x16x32_bf16 v[76:79], v[32:35], v[132:135], v[76:79]\n"
    "ds_read_b128 v[32:35], v0 offset:10240\n"
    "s_waitcnt lgkmcnt(6)\n"
    "v_mfma_f32_16x16x32_bf16 v[68:71], v[36:39], v[136:139], v[68:71]\n"
    "buffer_store_dword v104, v16, s[28:31], s80 offen nt\n"
    "v_mfma_f32_16x16x32_bf16 v[76:79], v[36:39], v[140:143], v[76:79]\n"
    "ds_read_b128 v[36:39], v3 offset:12288\n"
    "s_waitcnt lgkmcnt(6)\n"
    "v_mfma_f32_16x16x32_bf16 v[68:71], v[20:23], v[144:147], v[68:71]\n"
    "buffer_store_dword v105, v17, s[28:31], s80 offen nt\n"
    "v_mfma_f32_16x16x32_bf16 v[76:79], v[20:23], v[148:151], v[76:79]\n"
    "ds_read_b128 v[20:23], v0 offset:12288\n"
    "s_waitcnt lgkmcnt(6)\n"
    "v_mfma_f32_16x16x32_bf16 v[68:71], v[40:43], v[152:155], v[68:71]\n"
    "buffer_store_dword v106, v18, s[28:31], s80 offen nt\n"
    "v_mfma_f32_16x16x32_bf16 v[76:79], v[40:43], v[156:159], v[76:79]\n"
    "ds_read_b128 v[40:43], v3 offset:14336\n"
    "s_waitcnt lgkmcnt(6)\n"
    "v_mfma_f32_16x16x32_bf16 v[68:71], v[44:47], v[160:163], v[68:71]\n"
    "s_waitcnt vmcnt(62)\n"
    "buffer_store_dword v107, v19, s[28:31], s80 offen nt\n"
    "v_mfma_f32_16x16x32_bf16 v[76:79], v[44:47], v[164:167], v[76:79]\n"
    "ds_read_b128 v[44:47], v0 offset:14336\n"
    "s_waitcnt lgkmcnt(6)\n"
    "v_mfma_f32_16x16x32_bf16 v[68:71], v[8:11], v[168:171], v[68:71]\n"
    "s_waitcnt vmcnt(62)\n"
    "buffer_store_dword v52, v16, s[28:31], s77 offen nt\n"
    "v_mfma_f32_16x16x32_bf16 v[76:79], v[8:11], v[172:175], v[76:79]\n"
    "ds_read_b128 v[8:11], v3 offset:16384\n"
    "s_waitcnt lgkmcnt(6)\n"
    "v_mfma_f32_16x16x32_bf16 v[68:71], v[12:15], v[176:179], v[68:71]\n"
    "s_waitcnt vmcnt(62)\n"
    "buffer_store_dword v53, v17, s[28:31], s77 offen nt\n"
    "v_mfma_f32_16x16x32_bf16 v[76:79], v[12:15], v[180:183], v[76:79]\n"
    "ds_read_b128 v[12:15], v0 offset:16384\n"
    "s_waitcnt lgkmcnt(6)\n"
    "v_mfma_f32_16x16x32_bf16 v[68:71], v[32:35], v[184:187], v[68:71]\n"
    "s_waitcnt vmcnt(62)\n"
    "buffer_store_dword v54, v18, s[28:31], s77 offen nt\n"
    "v_mfma_f32_16x16x32_bf16 v[76:79], v[32:35], v[188:191], v[76:79]\n"
    "ds_read_b128 v[32:35], v3 offset:18432\n"
    "s_waitcnt lgkmcnt(6)\n"
    "v_mfma_f32_16x16x32_bf16 v[68:71], v[36:39], v[192:195], v[68:71]\n"
    "s_waitcnt vmcnt(62)\n"
    "buffer_store_dword v55, v19, s[28:31], s77 offen nt\n"
    "v_mfma_f32_16x16x32_bf16 v[76:79], v[36:39], v[196:199], v[76:79]\n"
    "ds_read_b128 v[36:39], v0 offset:18432\n"
    "s_cmp_lt_u32 s10, 2\n"
    "s_cbranch_scc1 L_M19_%=\n"
    "s_waitcnt lgkmcnt(6)\n"
    "v_mfma_f32_16x16x32_bf16 v[68:71], v[20:23], v[200:203], v[68:71]\n"
    "s_lshl_b32 s78, s67, 3\n"
    "v_mfma_f32_16x16x32_bf16 v[76:79], v[20:23], v[204:207], v[76:79]\n"
    "s_waitcnt lgkmcnt(5)\n"
    "v_mfma_f32_16x16x32_bf16 v[68:71], v[40:43], v[208:211], v[68:71]\n"
    "v_mfma_f32_16x16x32_bf16 v[76:79], v[40:43], v[212:215], v[76:79]\n"
    "s_waitcnt lgkmcnt(4)\n"
    "v_mfma_f32_16x16x32_bf16 v[68:71], v[44:47], v[216:219], v[68:71]\n"
    "v_mfma_f32_16x16x32_bf16 v[76:79], v[44:47], v[220:223], v[76:79]\n"
    "s_waitcnt lgkmcnt(3)\n"
    "v_mfma_f32_16x16x32_bf16 v[68:71], v[8:11], v[224:227], v[68:71]\n"
    "v_mfma_f32_16x16x32_bf16 v[76:79], v[8:11], v[228:231], v[76:79]\n"
    "s_waitcnt lgkmcnt(2)\n"
    "v_mfma_f32_16x16x32_bf16 v[68:71], v[12:15], v[232:235], v[68:71]\n"
    "v_mfma_f32_16x16x32_bf16 v[76:79], v[12:15], v[236:239], v[76:79]\n"
    "s_waitcnt lgkmcnt(1)\n"
    "v_mfma_f32_16x16x32_bf16 v[68:71], v[32:35], v[240:243], v[68:71]\n"
    "v_mfma_f32_16x16x32_bf16 v[76:79], v[32:35], v[244:247], v[76:79]\n"
    "s_waitcnt lgkmcnt(0)\n"
    "v_mfma_f32_16x16x32_bf16 v[68:71], v[36:39], v[248:251], v[68:71]\n"
    "v_mfma_f32_16x16x32_bf16 v[76:79], v[36:39], v[252:255], v[76:79]\n"
    "s_nop 7\n"
    "v_cvt_pk_bf16_f32 v68, v68, v76\n"
    "v_cvt_pk_bf16_f32 v69, v69, v77\n"
    "v_cvt_pk_bf16_f32 v70, v70, v78\n"
    "v_cvt_pk_bf16_f32 v71, v71, v79\n"
    "s_waitcnt vmcnt(62)\n"
    "buffer_store_dword v68, v16, s[28:31], s78 offen nt\n"
    "s_waitcnt vmcnt(62)\n"
    "buffer_store_dword v69, v17, s[28:31], s78 offen nt\n"
    "s_waitcnt vmcnt(62)\n"
    "buffer_store_dword v70, v18, s[28:31], s78 offen nt\n"
    "s_waitcnt vmcnt(62)\n"
    "buffer_store_dword v71, v19, s[28:31], s78 offen nt\n"
    "s_branch L_end_%=\n"
    "L_M19_%=:\n"
    "s_waitcnt lgkmcnt(6)\n"
    "v_mfma_f32_16x16x32_bf16 v[68:71], v[20:23], v[200:203], v[68:71]\n"
    "s_lshl_b32 s79, s67, 3\n"
    "v_mfma_f32_16x16x32_bf16 v[76:79], v[20:23], v[204:207], v[76:79]\n"
    "ds_read_b128 v[20:23], v3 offset:20480\n"
    "s_waitcnt lgkmcnt(6)\n"
    "v_mfma_f32_16x16x32_bf16 v[68:71], v[40:43], v[208:211], v[68:71]\n"
    "v_mfma_f32_16x16x32_bf16 v[76:79], v[40:43], v[212:215], v[76:79]\n"
    "ds_read_b128 v[40:43], v0 offset:20480\n"
    "s_waitcnt lgkmcnt(6)\n"
    "v_mfma_f32_16x16x32_bf16 v[68:71], v[44:47], v[216:219], v[68:71]\n"
    "v_mfma_f32_16x16x32_bf16 v[76:79], v[44:47], v[220:223], v[76:79]\n"
    "ds_read_b128 v[44:47], v3 offset:22528\n"
    "s_waitcnt lgkmcnt(6)\n"
    "v_mfma_f32_16x16x32_bf16 v[68:71], v[8:11], v[224:227], v[68:71]\n"
    "v_mfma_f32_16x16x32_bf16 v[76:79], v[8:11], v[228:231], v[76:79]\n"
    "ds_read_b128 v[8:11], v0 offset:22528\n"
    "s_waitcnt lgkmcnt(6)\n"
    "v_mfma_f32_16x16x32_bf16 v[68:71], v[12:15], v[232:235], v[68:71]\n"
    "v_mfma_f32_16x16x32_bf16 v[76:79], v[12:15], v[236:239], v[76:79]\n"
    "ds_read_b128 v[12:15], v3 offset:24576\n"
    "s_waitcnt lgkmcnt(6)\n"
    "v_mfma_f32_16x16x32_bf16 v[68:71], v[32:35], v[240:243], v[68:71]\n"
    "v_mfma_f32_16x16x32_bf16 v[76:79], v[32:35], v[244:247], v[76:79]\n"
    "ds_read_b128 v[32:35], v0 offset:24576\n"
    "s_waitcnt lgkmcnt(6)\n"
    "v_mfma_f32_16x16x32_bf16 v[68:71], v[36:39], v[248:251], v[68:71]\n"
    "v_mfma_f32_16x16x32_bf16 v[76:79], v[36:39], v[252:255], v[76:79]\n"
    "ds_read_b128 v[36:39], v3 offset:26624\n"
    "s_waitcnt lgkmcnt(6)\n"
    "v_mfma_f32_16x16x32_bf16 v[52:55], v[20:23], v[112:115], 0\n"
    "v_mfma_f32_16x16x32_bf16 v[60:63], v[20:23], v[116:119], 0\n"
    "ds_read_b128 v[20:23], v0 offset:26624\n"
    "s_waitcnt lgkmcnt(6)\n"
    "v_mfma_f32_16x16x32_bf16 v[52:55], v[40:43], v[120:123], v[52:55]\n"
    "s_nop 0\n"
    "v_cvt_pk_bf16_f32 v68, v68, v76\n"
    "v_cvt_pk_bf16_f32 v69, v69, v77\n"
    "v_mfma_f32_16x16x32_bf16 v[60:63], v[40:43], v[124:127], v[60:63]\n"
    "ds_read_b128 v[40:43], v3 offset:28672\n"
    "s_waitcnt lgkmcnt(6)\n"
    "v_mfma_f32_16x16x32_bf16 v[52:55], v[44:47], v[128:131], v[52:55]\n"
    "v_cvt_pk_bf16_f32 v70, v70, v78\n"
    "v_cvt_pk_bf16_f32 v71, v71, v79\n"
    "v_mfma_f32_16x16x32_bf16 v[60:63], v[44:47], v[132:135], v[60:63]\n"
    "ds_read_b128 v[44:47], v0 offset:28672\n"
    "s_waitcnt lgkmcnt(6)\n"
    "v_mfma_f32_16x16x32_bf16 v[52:55], v[8:11], v[136:139], v[52:55]\n"
    "s_waitcnt vmcnt(62)\n"
    "buffer_store_dword v68, v16, s[28:31], s79 offen nt\n"
    "v_mfma_f32_16x16x32_bf16 v[60:63], v[8:11], v[140:143], v[60:63]\n"
    "ds_read_b128 v[8:11], v3 offset:30720\n"
    "s_waitcnt lgkmcnt(6)\n"
    "v_mfma_f32_16x16x32_bf16 v[52:55], v[12:15], v[144:147], v[52:55]\n"
    "s_waitcnt vmcnt(62)\n"
    "buffer_store_dword v69, v17, s[28:31], s79 offen nt\n"
    "v_mfma_f32_16x16x32_bf16 v[60:63], v[12:15], v[148:151], v[60:63]\n"
    "ds_read_b128 v[12:15], v0 offset:30720\n"
    "s_waitcnt lgkmcnt(6)\n"
    "v_mfma_f32_16x16x32_bf16 v[52:55], v[32:35], v[152:155], v[52:55]\n"
    "s_waitcnt vmcnt(62)\n"
    "buffer_store_dword v70, v18, s[28:31], s79 offen nt\n"
    "v_mfma_f32_16x16x32_bf16 v[60:63], v[32:35], v[156:159], v[60:63]\n"
    "ds_read_b128 v[32:35], v3 offset:32768\n"
    "s_waitcnt lgkmcnt(6)\n"
    "v_mfma_f32_16x16x32_bf16 v[52:55], v[36:39], v[160:163], v[52:55]\n"
    "s_waitcnt vmcnt(62)\n"
    "buffer_store_dword v71, v19, s[28:31], s79 offen nt\n"
    "v_mfma_f32_16x16x32_bf16 v[60:63], v[36:39], v[164:167], v[60:63]\n"
    "ds_read_b128 v[36:39], v0 offset:32768\n"
    "s_waitcnt lgkmcnt(6)\n"
    "v_mfma_f32_16x16x32_bf16 v[52:55], v[20:23], v[168:171], v[52:55]\n"
    "v_mfma_f32_16x16x32_bf16 v[60:63], v[20:23], v[172:175], v[60:63]\n"
    "ds_read_b128 v[20:23], v3 offset:34816\n"
    "s_waitcnt lgkmcnt(6)\n"
    "v_mfma_f32_16x16x32_bf16 v[52:55], v[40:43], v[176:179], v[52:55]\n"
    "v_mfma_f32_16x16x32_bf16 v[60:63], v[40:43], v[180:183], v[60:63]\n"
    "ds_read_b128 v[40:43], v0 offset:34816\n"
    "s_waitcnt lgkmcnt(6)\n"
    "v_mfma_f32_16x16x32_bf16 v[52:55], v[44:47], v[184:187], v[52:55]\n"
    "v_mfma_f32_16x16x32_bf16 v[60:63], v[44:47], v[188:191], v[60:63]\n"
    "ds_read_b128 v[44:47], v3 offset:36864\n"
    "s_waitcnt lgkmcnt(6)\n"
    "v_mfma_f32_16x16x32_bf16 v[52:55], v[8:11], v[192:195], v[52:55]\n"
    "v_mfma_f32_16x16x32_bf16 v[60:63], v[8:11], v[196:199], v[60:63]\n"
    "ds_read_b128 v[8:11], v0 offset:36864\n"
    "s_waitcnt lgkmcnt(6)\n"
    "v_mfma_f32_16x16x32_bf16 v[52:55], v[12:15], v[200:203], v[52:55]\n"
    "s_lshl_b32 s80, s68, 3\n"
    "v_mfma_f32_16x16x32_bf16 v[60:63], v[12:15], v[204:207], v[60:63]\n"
    "s_waitcnt lgkmcnt(5)\n"
    "v_mfma_f32_16x16x32_bf16 v[52:55], v[32:35], v[208:211], v[52:55]\n"
    "v_mfma_f32_16x16x32_bf16 v[60:63], v[32:35], v[212:215], v[60:63]\n"
    "s_waitcnt lgkmcnt(4)\n"
    "v_mfma_f32_16x16x32_bf16 v[52:55], v[36:39], v[216:219], v[52:55]\n"
    "v_mfma_f32_16x16x32_bf16 v[60:63], v[36:39], v[220:223], v[60:63]\n"
    "s_waitcnt lgkmcnt(3)\n"
    "v_mfma_f32_16x16x32_bf16 v[52:55], v[20:23], v[224:227], v[52:55]\n"
    "v_mfma_f32_16x16x32_bf16 v[60:63], v[20:23], v[228:231], v[60:63]\n"
    "s_waitcnt lgkmcnt(2)\n"
    "v_mfma_f32_16x16x32_bf16 v[52:55], v[40:43], v[232:235], v[52:55]\n"
    "v_mfma_f32_16x16x32_bf16 v[60:63], v[40:43], v[236:239], v[60:63]\n"
    "s_waitcnt lgkmcnt(1)\n"
    "v_mfma_f32_16x16x32_bf16 v[52:55], v[44:47], v[240:243], v[52:55]\n"
    "v_mfma_f32_16x16x32_bf16 v[60:63], v[44:47], v[244:247], v[60:63]\n"
    "s_waitcnt lgkmcnt(0)\n"
    "v_mfma_f32_16x16x32_bf16 v[52:55], v[8:11], v[248:251], v[52:55]\n"
    "v_mfma_f32_16x16x32_bf16 v[60:63], v[8:11], v[252:255], v[60:63]\n"
    "s_nop 7\n"
    "v_cvt_pk_bf16_f32 v52, v52, v60\n"
    "v_cvt_pk_bf16_f32 v53, v53, v61\n"
    "v_cvt_pk_bf16_f32 v54, v54, v62\n"
    "v_cvt_pk_bf16_f32 v55, v55, v63\n"
    "s_waitcnt vmcnt(62)\n"
    "buffer_store_dword v52, v16, s[28:31], s80 offen nt\n"
    "s_waitcnt vmcnt(62)\n"
    "buffer_store_dword v53, v17, s[28:31], s80 offen nt\n"
    "s_waitcnt vmcnt(62)\n"
    "buffer_store_dword v54, v18, s[28:31], s80 offen nt\n"
    "s_waitcnt vmcnt(62)\n"
    "buffer_store_dword v55, v19, s[28:31], s80 offen nt\n"
    "L_end_%=:\n"
    : "+v"(tid)
    : "s"(__builtin_amdgcn_kernarg_segment_ptr()), "s"(blockIdx.x), "i"((__attribute__((address_space(3))) char*)lds)
    : "v1", "v2", "v3", "v4", "v5", "v6", "v7", "v8", "v9", "v10", "v11", "v12", "v13", "v14", "v15", "v16", "v17", "v18", "v19", "v20", "v21", "v22", "v23", "v24", "v25", "v26", "v27", "v28", "v29", "v30", "v31", "v32", "v33", "v34", "v35", "v36", "v37", "v38", "v39", "v40", "v41", "v42", "v43", "v44", "v45", "v46", "v47", "v48", "v49", "v50", "v51", "v52", "v53", "v54", "v55", "v56", "v57", "v58", "v59", "v60", "v61", "v62", "v63", "v64", "v65", "v66", "v67", "v68", "v69", "v70", "v71", "v72", "v73", "v74", "v75", "v76", "v77", "v78", "v79", "v80", "v81", "v82", "v83", "v84", "v85", "v86", "v87", "v88", "v89", "v90", "v91", "v92", "v93", "v94", "v95", "v96", "v97", "v98", "v99", "v100", "v101", "v102", "v103", "v104", "v105", "v106", "v107", "v108", "v109", "v110", "v111", "v112", "v113", "v114", "v115", "v116", "v117", "v118", "v119", "v120", "v121", "v122", "v123", "v124", "v125", "v126", "v127", "v128", "v129", "v130", "v131", "v132", "v133", "v134", "v135", "v136", "v137", "v138", "v139", "v140", "v141", "v142", "v143", "v144", "v145", "v146", "v147", "v148", "v149", "v150", "v151", "v152", "v153", "v154", "v155", "v156", "v157", "v158", "v159", "v160", "v161", "v162", "v163", "v164", "v165", "v166", "v167", "v168", "v169", "v170", "v171", "v172", "v173", "v174", "v175", "v176", "v177", "v178", "v179", "v180", "v181", "v182", "v183", "v184", "v185", "v186", "v187", "v188", "v189", "v190", "v191", "v192", "v193", "v194", "v195", "v196", "v197", "v198", "v199", "v200", "v201", "v202", "v203", "v204", "v205", "v206", "v207", "v208", "v209", "v210", "v211", "v212", "v213", "v214", "v215", "v216", "v217", "v218", "v219", "v220", "v221", "v222", "v223", "v224", "v225", "v226", "v227", "v228", "v229", "v230", "v231", "v232", "v233", "v234", "v235", "v236", "v237", "v238", "v239", "v240", "v241", "v242", "v243", "v244", "v245", "v246", "v247", "v248", "v249", "v250", "v251", "v252", "v253", "v254", "v255", "s6", "s7", "s8", "s9", "s10", "s11", "s12", "s13", "s14", "s15", "s16", "s17", "s18", "s19", "s20", "s21", "s22", "s23", "s24", "s25", "s26", "s27", "s28", "s29", "s30", "s31", "s36", "s37", "s38", "s39", "s40", "s41", "s42", "s43", "s44", "s45", "s46", "s47", "s48", "s49", "s50", "s51", "s52", "s53", "s54", "s55", "s56", "s57", "s58", "s59", "s60", "s61", "s62", "s63", "s64", "s65", "s66", "s67", "s68", "s69", "s70", "s71", "s72", "s73", "s74", "s75", "s76", "s77", "s78", "s79", "s80", "s81", "s82", "s83", "s84", "s85", "s86", "s87", "s88", "s89", "s90", "s91", "s92", "s93", "s94", "s95", "s96", "s97", "s98", "s99", "vcc", "scc", "memory");
}

extern "C" void kernel_launch(void* const* d_in, const int* in_sizes, int n_in,
                              void* d_out, int out_size, void* d_ws, size_t ws_size,
                              hipStream_t stream) {
  const unsigned F = 0x00020000u;
  sqllm_gemm_kernel<<<256, 512, 0, stream>>>(d_in[0], (unsigned)in_sizes[0] * 2u, F, d_in[1], (unsigned)in_sizes[1] * 4u, F,
                                             d_in[2], (unsigned)in_sizes[2] * 2u, F, d_out, (unsigned)out_size * 2u, F);
}
```
